```python
import torch

def _softmax(x, axis=-1):
    return torch.softmax(x, dim=axis)

def _gelu(x):
    return torch.nn.functional.gelu(x, approximate='tanh')

def _silu(x):
    return torch.nn.functional.silu(x)

def _sigmoid(x):
    return torch.sigmoid(x)

def _layernorm(x, axis=-1, eps=1e-05):
    m = x.mean(dim=axis, keepdim=True)
    v = x.var(dim=axis, keepdim=True, unbiased=False)
    return (x - m) / torch.sqrt(v + eps)

def _scatter_add(tgt, idx, src, axis=0):
    return tgt.clone().index_add_(axis, idx, src)

def _segment_sum(src, seg, n):
    out = torch.zeros((n,) + src.shape[1:], dtype=src.dtype)
    return out.index_add_(0, seg, src)

def _pad(x, pw):
    flat = tuple((v for pair in reversed(pw) for v in pair))
    return torch.nn.functional.pad(x, flat)
kernel_name = 'interleave_layers__ssm_conv_h3__memory_aug_recurrent_mem_tr__prompt'

def setup_inputs():

    _w = lambda a: (lambda t: t.to(torch.bfloat16) if t.is_floating_point() else t)(torch.as_tensor(a))

    def _A_setup():
        B, S, D = (1, 2048, 448)
        ks = 4
        return {'q': _w((lambda *s: torch.randn(*s, dtype=torch.bfloat16) * 0.05)(B, S, D)), 'k': _w((lambda *s: torch.randn(*s, dtype=torch.bfloat16) * 0.2)(B, S, D)), 'v': _w((lambda *s: torch.randn(*s, dtype=torch.bfloat16) * 0.2)(B, S, D)), 'shift_k': _w((lambda *s: torch.randn(*s, dtype=torch.bfloat16) * 0.2)(D, ks) * 0.5), 'ssm_k': _w((lambda *s: torch.randn(*s, dtype=torch.bfloat16) * 0.2)(D, S) * (1.0 / S ** 0.5)), 'ssm_D': _w((lambda *s: torch.randn(*s, dtype=torch.bfloat16) * 0.2)(D))}
    _A_in = _A_setup()

    def _B_setup():
        B, S, Dm = (1, 2048, 448)
        H, Dh = (16, 256)
        M, seg = (8, 32)
        seg = min(seg, S)
        S = S // seg * seg
        return {'x': _w((lambda *s: torch.randn(*s, dtype=torch.bfloat16) * 0.05)(B, S, Dm)), 'mem0': _w((lambda *s: torch.randn(*s, dtype=torch.bfloat16) * 0.2)(B, M, Dm)), 'Wq': _w((lambda *s: torch.randn(*s, dtype=torch.bfloat16) * 1.6)(Dm, H, Dh) * Dm ** (-0.5)), 'Wk': _w((lambda *s: torch.randn(*s, dtype=torch.bfloat16) * 1.6)(Dm, H, Dh) * Dm ** (-0.5)), 'Wv': _w((lambda *s: torch.randn(*s, dtype=torch.bfloat16) * 0.2)(Dm, H, Dh) * Dm ** (-0.5)), 'Wo': _w((lambda *s: torch.randn(*s, dtype=torch.bfloat16) * 0.2)(H, Dh, Dm) * (H * Dh) ** (-0.5)), 'seg_len': seg}
    _B_in = _B_setup()
    return {'A_q': _A_in['q'], 'A_k': _A_in['k'], 'A_v': _A_in['v'], 'A_shift_k': _A_in['shift_k'], 'A_ssm_k': _A_in['ssm_k'], 'A_ssm_D': _A_in['ssm_D'], 'B_x': _B_in['x'], 'B_mem0': _B_in['mem0'], 'B_Wq': _B_in['Wq'], 'B_Wk': _B_in['Wk'], 'B_Wv': _B_in['Wv'], 'B_Wo': _B_in['Wo'], 'B_seg_len': _B_in['seg_len']}

def reference(A_q, A_k, A_v, A_shift_k, A_ssm_k, A_ssm_D, B_x, B_mem0, B_Wq, B_Wk, B_Wv, B_Wo, B_seg_len):

    def _A_ref(q, k, v, shift_k, ssm_k, ssm_D):
        B, S, D = q.shape
        ks = shift_k.shape[-1]
        k_pad = _pad(k, ((0, 0), (ks - 1, 0), (0, 0)))
        k_shift = torch.zeros_like(k)
        for t in range(ks):
            k_shift = k_shift + k_pad[:, t:t + S, :] * shift_k[:, ks - 1 - t][None, None, :]
        kv = k_shift * v
        L = 2 * S
        kv_pad = _pad(kv, ((0, 0), (0, S), (0, 0)))
        kf_pad = _pad(ssm_k, ((0, 0), (0, S)))
        kv_f = (lambda x, axes=(-2, -1): torch.fft.rfft2(x.float(), dim=axes))(kv_pad[:, None, :, :], axes=(1, 2))
        kf_f = (lambda x, axes=(-2, -1): torch.fft.rfft2(x.float(), dim=axes))(kf_pad[:, None, :], axes=(1, 2))
        kf_f = (lambda x, *d: x.permute(*d))(kf_f, 1, 2, 0)
        y_f = kv_f * kf_f[None, :, :, :]
        y = (lambda x, s, axes=(-2, -1): torch.fft.irfft2(x, s=s, dim=axes).to(torch.bfloat16))(y_f, (1, L), axes=(1, 2))[:, 0, :S, :]
        y = y + kv * ssm_D[None, None, :]
        return q * y

    def _B_ref(x, mem0, Wq, Wk, Wv, Wo, seg_len):
        B, S, Dm = x.shape
        H, Dh = (Wq.shape[1], Wq.shape[2])
        M = mem0.shape[1]
        scale = Dh ** (-0.5)
        n_seg = S // seg_len
        L = M + seg_len
        idx = torch.arange(L)
        mask = (idx[:, None] < M) | (idx[:, None] >= idx[None, :])
        mem = mem0
        outs = []
        for s in range(n_seg):
            xs = x[:, s * seg_len:(s + 1) * seg_len, :].clone()
            h = torch.concatenate([mem, xs], axis=1)
            q = torch.einsum('btd,dhe->bhte', h, Wq)
            k = torch.einsum('btd,dhe->bhte', h, Wk)
            v = torch.einsum('btd,dhe->bhte', h, Wv)
            scores = torch.einsum('bhqe,bhke->bhqk', q, k) * scale
            scores = torch.where(mask[None, None], scores, -1000000000.0)
            p = _softmax(scores, axis=-1)
            o = torch.einsum('bhqk,bhke->bhqe', p, v)
            o = torch.einsum('bhte,hed->btd', o, Wo)
            mem = o[:, :M, :].clone()
            outs.append(o[:, M:, :])
        return torch.concatenate(outs, axis=1)
    x = A_q
    x = x + _A_ref(_layernorm(x, axis=-1), A_k, A_v, A_shift_k, A_ssm_k, A_ssm_D)
    x = x + _B_ref(_layernorm(x, axis=-1), B_mem0, B_Wq, B_Wk, B_Wv, B_Wo, B_seg_len)
    x = x + _A_ref(_layernorm(x, axis=-1), A_k, A_v, A_shift_k, A_ssm_k, A_ssm_D)
    x = x + _B_ref(_layernorm(x, axis=-1), B_mem0, B_Wq, B_Wk, B_Wv, B_Wo, B_seg_len)
    return x
```

```cpp
#include <hip/hip_runtime.h>
#include <hip/hip_bf16.h>
#include <stdint.h>

typedef __attribute__((ext_vector_type(8))) short bf16x8;
typedef __attribute__((ext_vector_type(4))) short bf16x4;
typedef __attribute__((ext_vector_type(4))) float f32x4;
typedef __attribute__((ext_vector_type(16))) float f32x16;
typedef __attribute__((ext_vector_type(4))) unsigned u32x4;
typedef __attribute__((ext_vector_type(2))) unsigned u32x2;
typedef __attribute__((ext_vector_type(2))) float f32x2;
typedef __attribute__((ext_vector_type(2))) __bf16 bf2;
#define DI __device__ __forceinline__
#define GAS __attribute__((address_space(1)))

constexpr int DM = 448, SEQ = 2048, NH = 16, NSEG = 64, LAG = 5;
constexpr int NTICK = NSEG + LAG;
constexpr int NTEAM = 13;
constexpr int NV = 28;
constexpr int RS = 912;
constexpr int MEMW = NV * 128;

constexpr size_t SZ_F = (size_t)NH * 28 * 14 * 1024;
constexpr size_t SZ_X = (size_t)SEQ * DM * 2;
constexpr size_t OFF_AF = 0;
constexpr size_t OFF_BF = OFF_AF + SZ_F;
constexpr size_t OFF_Y = OFF_BF + SZ_F;
constexpr size_t OFF_X1 = OFF_Y + SZ_X;
constexpr size_t OFF_XS1 = OFF_X1 + SZ_X;
constexpr size_t OFF_X2 = OFF_XS1 + SZ_X;
constexpr size_t OFF_XS2 = OFF_X2 + SZ_X;
constexpr size_t OFF_Z = OFF_XS2 + SZ_X;
constexpr size_t OFF_MEM = OFF_Z;
constexpr size_t SZ_MEM = (size_t)(NTICK + 1) * MEMW * 4;
constexpr size_t OFF_PB = OFF_MEM + SZ_MEM;
constexpr size_t SZ_PB = (size_t)NTICK * NH * 384 * 4;
constexpr size_t OFF_CNT = OFF_PB + SZ_PB;
constexpr size_t SZ_CNT = (size_t)(128 + 64 + 8) * 128;
constexpr size_t OFF_ZEND = OFF_CNT + SZ_CNT;
constexpr size_t OFF_PART = (OFF_ZEND + 4095) / 4096 * 4096;
constexpr size_t SZ_PART1 = (size_t)28 * 2 * 64 * 16;

DI float bf2f(unsigned short u) { return __uint_as_float((unsigned)u << 16); }
DI unsigned short f2bf(float f) { __bf16 b = (__bf16)f; return __builtin_bit_cast(unsigned short, b); }
DI float rbf(float f) { return bf2f(f2bf(f)); }
DI unsigned pk2(float a, float b) { bf2 v = {(__bf16)a, (__bf16)b}; return __builtin_bit_cast(unsigned, v); }
DI f32x4 mfma32(bf16x8 a, bf16x8 b, f32x4 c) { return __builtin_amdgcn_mfma_f32_16x16x32_bf16(a, b, c, 0, 0, 0); }
DI f32x4 mfma16(bf16x4 a, bf16x4 b, f32x4 c) {
  const bf16x8 a8 = {a[0], a[1], a[2], a[3], 0, 0, 0, 0}, b8 = {b[0], b[1], b[2], b[3], 0, 0, 0, 0};
  return __builtin_amdgcn_mfma_f32_16x16x32_bf16(a8, b8, c, 0, 0, 0);
}
DI bf16x4 pack4(f32x4 v) { u32x2 p = {pk2(v[0], v[1]), pk2(v[2], v[3])}; return __builtin_bit_cast(bf16x4, p); }
DI bf16x8 pack8(f32x4 a, f32x4 b) { u32x4 p = {pk2(a[0], a[1]), pk2(a[2], a[3]), pk2(b[0], b[1]), pk2(b[2], b[3])}; return __builtin_bit_cast(bf16x8, p); }

typedef __amdgpu_buffer_rsrc_t rsrc_t;
constexpr int AUX_SC1 = 16;
DI rsrc_t make_rsrc(const void* p) { return __builtin_amdgcn_make_buffer_rsrc((void*)p, (short)0, (int)0x10000000, 0x00020000); }
DI u32x4 ld16(rsrc_t r, unsigned off) { return __builtin_amdgcn_raw_buffer_load_b128(r, off, 0, AUX_SC1); }
DI u32x2 ld8(rsrc_t r, unsigned off) { return __builtin_amdgcn_raw_buffer_load_b64(r, off, 0, AUX_SC1); }
DI unsigned short ld2(rsrc_t r, unsigned off) { return __builtin_amdgcn_raw_buffer_load_b16(r, off, 0, AUX_SC1); }
DI void st16(rsrc_t r, unsigned off, u32x4 v) { __builtin_amdgcn_raw_buffer_store_b128(v, r, off, 0, AUX_SC1); }
DI void st8(rsrc_t r, unsigned off, u32x2 v) { __builtin_amdgcn_raw_buffer_store_b64(v, r, off, 0, AUX_SC1); }
DI void st2(rsrc_t r, unsigned off, unsigned short v) { __builtin_amdgcn_raw_buffer_store_b16(v, r, off, 0, AUX_SC1); }
DI void ldw7(bf16x8 (&d)[7], unsigned v0, unsigned v1, const char* base) {
  asm volatile(
      "s_nop 4\n"
      "global_load_dwordx4 %0, %7, %9\n"
      "global_load_dwordx4 %1, %7, %9 offset:1024\n"
      "global_load_dwordx4 %2, %7, %9 offset:2048\n"
      "global_load_dwordx4 %3, %7, %9 offset:3072\n"
      "global_load_dwordx4 %4, %8, %9\n"
      "global_load_dwordx4 %5, %8, %9 offset:1024\n"
      "global_load_dwordx4 %6, %8, %9 offset:2048\n"
      : "=&v"(d[0]), "=&v"(d[1]), "=&v"(d[2]), "=&v"(d[3]), "=&v"(d[4]), "=&v"(d[5]), "=&v"(d[6])
      : "v"(v0), "v"(v1), "s"(base));
}
template <int N> DI void waitw7(bf16x8 (&d)[7]) {
  asm volatile("s_waitcnt vmcnt(%7)" : "+v"(d[0]), "+v"(d[1]), "+v"(d[2]), "+v"(d[3]), "+v"(d[4]), "+v"(d[5]), "+v"(d[6]) : "n"(N));
}
DI void fence_c() { asm volatile("" ::: "memory"); }
DI void wait_vm0() { asm volatile("s_waitcnt vmcnt(0)" ::: "memory"); }
DI bool nz4(u32x4 v) { return v[0] && v[1] && v[2] && v[3]; }
DI unsigned ld_cnt(const unsigned* p) { return __hip_atomic_load((const GAS unsigned*)p, __ATOMIC_RELAXED, __HIP_MEMORY_SCOPE_AGENT); }
DI void add_cnt(unsigned* p) { __hip_atomic_fetch_add((GAS unsigned*)p, 1u, __ATOMIC_RELAXED, __HIP_MEMORY_SCOPE_AGENT); }
#define SPIN_DECL
#define SPIN_CHK(ws, code)
DI void wait_cnt(const unsigned* p, unsigned target, const char* ws) {
  SPIN_DECL;
  while (ld_cnt(p) < target) { __builtin_amdgcn_s_sleep(2); SPIN_CHK((char*)ws, 1); }
}

constexpr int HC_STRIDE = 2192;
__global__ void __launch_bounds__(256) k_pre(const unsigned short* __restrict__ gk, const unsigned short* __restrict__ gv,
                                             const unsigned short* __restrict__ gshift, const unsigned short* __restrict__ gssmk,
                                             const unsigned short* __restrict__ gD, const unsigned short* __restrict__ Wq,
                                             const unsigned short* __restrict__ Wk, const unsigned short* __restrict__ Wv,
                                             const unsigned short* __restrict__ Wo, char* __restrict__ ws) {
  extern __shared__ __attribute__((aligned(16))) char smem[];
  const int tid = threadIdx.x, lane = tid & 63, w = __builtin_amdgcn_readfirstlane(tid >> 6);
  if (blockIdx.x < 224) {
    const int d0 = blockIdx.x * 2;
    unsigned short* hc = (unsigned short*)smem;
    unsigned short* kvz = hc + 2 * 8 * HC_STRIDE;
    unsigned* kraw = (unsigned*)(kvz + 2 * 4032);
    if (tid < 4) kraw[tid] = 0;
    for (int t = tid; t < SEQ; t += 256) kraw[4 + t] = *(const unsigned*)(gk + (size_t)t * DM + d0);
    for (int i = tid; i < 2 * 1984; i += 256) { int ch = i / 1984; kvz[ch * 4032 + (i % 1984)] = 0; }
    for (int ch = 0; ch < 2; ++ch) {
      const unsigned short* hsrc = gssmk + (size_t)(d0 + ch) * SEQ;
      for (int i = tid; i < 8 * 2112; i += 256) {
        int q = i / 2112, u = i % 2112;
        int p = u + q;
        int idx = 2047 - p;
        hc[(ch * 8 + q) * HC_STRIDE + u] = (idx >= 0) ? hsrc[idx] : (unsigned short)0;
      }
    }
    __syncthreads();
    {
      float sh[2][4];
      for (int ch = 0; ch < 2; ++ch)
        for (int j = 0; j < 4; ++j) sh[ch][j] = bf2f(gshift[(d0 + ch) * 4 + j]);
      for (int t = tid; t < SEQ; t += 256) {
        unsigned vv = *(const unsigned*)(gv + (size_t)t * DM + d0);
#pragma unroll
        for (int ch = 0; ch < 2; ++ch) {
          float acc = 0.f;
#pragma unroll
          for (int j = 0; j < 4; ++j) {
            unsigned kk = kraw[4 + t - 3 + j];
            float kf = bf2f((unsigned short)(ch ? (kk >> 16) : (kk & 0xffff)));
            acc = rbf(acc + rbf(kf * sh[ch][3 - j]));
          }
          float vf = bf2f((unsigned short)(ch ? (vv >> 16) : (vv & 0xffff)));
          kvz[ch * 4032 + 1984 + t] = f2bf(acc * vf);
        }
      }
    }
    __syncthreads();
    {
      const int ch = w >> 1, mt = w & 1;
      const int n = lane & 31, hf = lane >> 5;
      const int b = 32 * mt + n;
      f32x16 acc;
#pragma unroll
      for (int i = 0; i < 16; ++i) acc[i] = 0.f;
      const unsigned short* hcc = hc + ch * 8 * HC_STRIDE;
      const unsigned short* kz = kvz + ch * 4032 + 1984;
      for (int c = 0; c < 32; ++c) {
#pragma unroll
        for (int ks = 0; ks < 4; ++ks) {
          const int j0 = 16 * ks + 8 * hf;
          const int p0 = 2047 - 64 * c - b + j0;
          const int q = p0 & 7, u = p0 - q;
          bf16x8 a = *(const bf16x8*)(hcc + q * HC_STRIDE + u);
          bf16x8 bb = *(const bf16x8*)(kz + 64 * (n - c) + j0);
          acc = __builtin_amdgcn_mfma_f32_32x32x16_bf16(a, bb, acc, 0, 0, 0);
        }
      }
      const float Dv = bf2f(gD[d0 + ch]);
      unsigned short* y = (unsigned short*)(ws + OFF_Y);
#pragma unroll
      for (int r = 0; r < 16; ++r) {
        const int row = (r & 3) + 8 * (r >> 2) + 4 * hf;
        const int t = 64 * n + 32 * mt + row;
        const float kvv = bf2f(kz[t]);
        const float yy = rbf(rbf(acc[r]) + rbf(kvv * Dv));
        y[(size_t)t * DM + d0 + ch] = f2bf(yy);
      }
    }
  } else {
    const int unit = (blockIdx.x - 224) * 4 + w;
    const int mat = unit / 448, rem = unit % 448, h = rem / 28, ct = rem % 28;
    const int n = lane & 15, g = lane >> 4;
    bf16x8 bfr[8];
    if (mat == 0) {
      const unsigned short* p = Wk + ((size_t)(16 * ct + n) * NH + h) * 256 + 8 * g;
#pragma unroll
      for (int es = 0; es < 8; ++es) bfr[es] = *(const bf16x8*)(p + 32 * es);
    } else {
      const unsigned short* p = Wo + ((size_t)h * 256 + 8 * g) * DM + 16 * ct + n;
#pragma unroll
      for (int es = 0; es < 8; ++es)
#pragma unroll
        for (int i = 0; i < 8; ++i) bfr[es][i] = (short)p[(size_t)(32 * es + i) * DM];
    }
    const unsigned short* Wa = mat == 0 ? Wq : Wv;
    unsigned short* dst = (unsigned short*)(ws + (mat == 0 ? OFF_AF : OFF_BF)) + ((size_t)(h * 28 + ct) * 14) * 512;
    for (int kt = 0; kt < 28; ++kt) {
      const unsigned short* pa = Wa + ((size_t)(16 * kt + n) * NH + h) * 256 + 8 * g;
      f32x4 acc = {0.f, 0.f, 0.f, 0.f};
#pragma unroll
      for (int es = 0; es < 8; ++es) acc = mfma32(*(const bf16x8*)(pa + 32 * es), bfr[es], acc);
      const int ks = kt >> 1, gp = 2 * (kt & 1) + (g >> 1), hf = g & 1;
      *(bf16x4*)(dst + (size_t)ks * 512 + (gp * 16 + n) * 8 + hf * 4) = pack4(acc);
    }
  }
}

DI float wave_sum(float v) {
#pragma unroll
  for (int o = 32; o > 0; o >>= 1) v += __shfl_xor(v, o);
  return v;
}

__global__ void __launch_bounds__(256) k_ln(const unsigned short* __restrict__ xin, char* __restrict__ ws) {
  const int row = blockIdx.x * 4 + (threadIdx.x >> 6), lane = threadIdx.x & 63;
  const unsigned short* y = (const unsigned short*)(ws + OFF_Y) + (size_t)row * DM;
  const unsigned short* x = xin + (size_t)row * DM;
  unsigned short* x1 = (unsigned short*)(ws + OFF_X1) + (size_t)row * DM;
  unsigned short* xs1 = (unsigned short*)(ws + OFF_XS1) + (size_t)row * DM;
  float v[7];
#pragma unroll
  for (int i = 0; i < 7; ++i) v[i] = bf2f(x[lane + 64 * i]);
#pragma unroll
  for (int pass = 0; pass < 2; ++pass) {
    float s = 0.f;
#pragma unroll
    for (int i = 0; i < 7; ++i) s += v[i];
    const float mean = wave_sum(s) * (1.f / DM);
    float q = 0.f;
#pragma unroll
    for (int i = 0; i < 7; ++i) q += (v[i] - mean) * (v[i] - mean);
    const float var = wave_sum(q) * (1.f / DM);
    const float mb = rbf(mean), den = rbf(sqrtf(rbf(rbf(var) + 1e-5f)));
    if (pass == 0) {
#pragma unroll
      for (int i = 0; i < 7; ++i) {
        const float ln = rbf(rbf(v[i] - mb) / den);
        v[i] = rbf(v[i] + rbf(ln * bf2f(y[lane + 64 * i])));
        x1[lane + 64 * i] = f2bf(v[i]);
      }
    } else {
#pragma unroll
      for (int i = 0; i < 7; ++i) xs1[lane + 64 * i] = f2bf(rbf(v[i] - mb) / den);
    }
  }
}

DI void gather_mem(rsrc_t rs, unsigned slot, char* lmem, int tid, char* ws) {
  u32x4 v[4];
  unsigned p[4];
#pragma unroll
  for (int r = 0; r < 4; ++r) { int c = tid + 256 * r; if (c >= 896) c = tid; p[r] = slot + c * 16; }
  bool all;
  SPIN_DECL;
  do {
    fence_c();
#pragma unroll
    for (int r = 0; r < 4; ++r) v[r] = ld16(rs, p[r]);
    all = true;
#pragma unroll
    for (int r = 0; r < 4; ++r) all = all && nz4(v[r]);
    SPIN_CHK(ws, 2);
  } while (!all);
#pragma unroll
  for (int r = 0; r < 4; ++r) {
    const int c = tid + 256 * r;
    if (c < 896) {
      const int vv = c >> 5, row = (c & 31) >> 1, hf = c & 1;
      *(u32x4*)(lmem + row * RS + (vv * 16 + hf * 8) * 2) = ~v[r];
    }
  }
}

DI void load_xs(rsrc_t rs, const char* ws, char* lxs, int tick, int t, int nthr) {
  const int s1 = tick < NSEG ? tick : 0;
  int s2 = tick - LAG;
  const bool v2 = s2 >= 0;
  const unsigned src1 = (unsigned)OFF_XS1 + s1 * 32 * DM * 2;
  const unsigned src2 = v2 ? (unsigned)OFF_XS2 + s2 * 32 * DM * 2 : (unsigned)OFF_XS1;
  if (v2) wait_cnt((const unsigned*)(ws + OFF_CNT) + (128 + s2) * 32, 8, ws);
  fence_c();
  for (int c0 = t; c0 < 3584; c0 += nthr * 4) {
    u32x4 v[4];
#pragma unroll
    for (int r = 0; r < 4; ++r) {
      int c = c0 + r * nthr; if (c >= 3584) c = t;
      const int row = c / 56, q = c % 56;
      v[r] = ld16(rs, (row < 32 ? src1 + row * DM * 2 : src2 + (row - 32) * DM * 2) + q * 16);
    }
#pragma unroll
    for (int r = 0; r < 4; ++r) {
      const int c = c0 + r * nthr;
      if (c < 3584) { const int row = c / 56, q = c % 56; *(u32x4*)(lxs + row * RS + q * 16) = v[r]; }
    }
  }
}

DI void role_S(char* ws, char* smem, int h) {
  const int tid = threadIdx.x, lane = tid & 63, w = __builtin_amdgcn_readfirstlane(tid >> 6);
  const int n = lane & 15, g = lane >> 4;
  char* lmem = smem;
  char* lxs = smem + 16 * RS;
  char* lw = lxs + 64 * RS;
  float* red = (float*)(lw + 4 * 14 * 1024);
  const rsrc_t rs = make_rsrc(ws);
  const char* Af = ws + OFF_AF + (size_t)h * 28 * 14 * 1024;
  bf16x8 W[6][14];
#pragma unroll
  for (int c = 0; c < 6; ++c)
#pragma unroll
    for (int ks = 0; ks < 14; ++ks) W[c][ks] = *(const bf16x8*)(Af + ((size_t)(7 * w + c) * 14 + ks) * 1024 + lane * 16);
#pragma unroll
  for (int ks = 0; ks < 14; ++ks)
    *(bf16x8*)(lw + (w * 14 + ks) * 1024 + lane * 16) = *(const bf16x8*)(Af + ((size_t)(7 * w + 6) * 14 + ks) * 1024 + lane * 16);
  load_xs(rs, ws, lxs, 0, tid, 256);
  for (int tick = 0; tick < NTICK; ++tick) {
    gather_mem(rs, (unsigned)OFF_MEM + tick * MEMW * 4, lmem, tid, ws);
    __syncthreads();
    f32x4 acc[7];
#pragma unroll
    for (int c = 0; c < 7; ++c) acc[c] = (f32x4){0.f, 0.f, 0.f, 0.f};
#pragma unroll
    for (int ks = 0; ks < 14; ++ks) {
      const bf16x8 mf = *(const bf16x8*)(lmem + n * RS + (32 * ks + 8 * g) * 2);
#pragma unroll
      for (int c = 0; c < 6; ++c) acc[c] = mfma32(W[c][ks], mf, acc[c]);
      const bf16x8 wl = *(const bf16x8*)(lw + (w * 14 + ks) * 1024 + lane * 16);
      acc[6] = mfma32(wl, mf, acc[6]);
    }
    bf16x8 tA[3];
#pragma unroll
    for (int p = 0; p < 3; ++p) tA[p] = pack8(acc[2 * p], acc[2 * p + 1]);
    const bf16x4 tB = pack4(acc[6]);
    f32x4 sc[5];
#pragma unroll
    for (int nt = 0; nt < 5; ++nt) {
      const char* rowp = (nt == 0 ? lmem + n * RS : lxs + (16 * (nt - 1) + n) * RS) + (112 * w + 4 * g) * 2;
      f32x4 s = {0.f, 0.f, 0.f, 0.f};
#pragma unroll
      for (int p = 0; p < 3; ++p) {
        u32x2 lo = *(const u32x2*)(rowp + 64 * p), hi = *(const u32x2*)(rowp + 64 * p + 32);
        u32x4 kb = {lo[0], lo[1], hi[0], hi[1]};
        s = mfma32(tA[p], __builtin_bit_cast(bf16x8, kb), s);
      }
      u32x2 lo = *(const u32x2*)(rowp + 192);
      s = mfma16(tB, __builtin_bit_cast(bf16x4, lo), s);
      sc[nt] = s;
    }
#pragma unroll
    for (int nt = 0; nt < 5; ++nt)
#pragma unroll
      for (int i = 0; i < 4; ++i) red[((w * 5 + nt) * 16 + 4 * g + i) * 16 + n] = sc[nt][i];
    __syncthreads();
    if (w == 0) {
      const int r = n, l2 = r >> 3;
      f32x4 sm = {0.f, 0.f, 0.f, 0.f}, sa = sm, sb = sm;
#pragma unroll
      for (int ww = 0; ww < 4; ++ww) {
        sm += *(const f32x4*)(red + ((ww * 5 + 0) * 16 + r) * 16 + 4 * g);
        sa += *(const f32x4*)(red + ((ww * 5 + 1 + 2 * l2) * 16 + r) * 16 + 4 * g);
        sb += *(const f32x4*)(red + ((ww * 5 + 2 + 2 * l2) * 16 + r) * 16 + 4 * g);
      }
      const bool mv = (g >> 1) == l2;
      float mx = -3.0e38f;
#pragma unroll
      for (int i = 0; i < 4; ++i) { sm[i] *= 0.0625f; sa[i] *= 0.0625f; sb[i] *= 0.0625f; if (mv) mx = fmaxf(mx, sm[i]); mx = fmaxf(mx, fmaxf(sa[i], sb[i])); }
      mx = fmaxf(mx, __shfl_xor(mx, 16)); mx = fmaxf(mx, __shfl_xor(mx, 32));
      float sum = 0.f;
#pragma unroll
      for (int i = 0; i < 4; ++i) {
        sm[i] = mv ? __expf(sm[i] - mx) : 0.f; sa[i] = __expf(sa[i] - mx); sb[i] = __expf(sb[i] - mx);
        sum += sm[i] + sa[i] + sb[i];
      }
      sum += __shfl_xor(sum, 16); sum += __shfl_xor(sum, 32);
      const float inv = 1.f / sum;
      const unsigned pb = (unsigned)OFF_PB + (tick * NH + h) * 384 * 4;
      u32x2 pm = {~pk2(sm[0] * inv, sm[1] * inv), ~pk2(sm[2] * inv, sm[3] * inv)};
      u32x4 px = {~pk2(sa[0] * inv, sa[1] * inv), ~pk2(sa[2] * inv, sa[3] * inv), ~pk2(sb[0] * inv, sb[1] * inv), ~pk2(sb[2] * inv, sb[3] * inv)};
      st16(rs, pb + 512 + lane * 16, px);
      st8(rs, pb + lane * 8, pm);
    } else if (tick + 1 < NTICK) {
      load_xs(rs, ws, lxs, tick + 1, tid - 64, 192);
    }
  }
}

DI void role_V(char* ws, char* smem, int v, const unsigned short* mem0) {
  const int tid = threadIdx.x, lane = tid & 63, w = __builtin_amdgcn_readfirstlane(tid >> 6);
  const int n = lane & 15, g = lane >> 4;
  char* lmem = smem;
  char* lxs0 = smem + 16 * RS;
  float* red = (float*)(lxs0 + 128 * RS);
  const rsrc_t rs = make_rsrc(ws);
  bf16x8 W[4][14];
#pragma unroll
  for (int hh = 0; hh < 4; ++hh)
#pragma unroll
    for (int ks = 0; ks < 14; ++ks) W[hh][ks] = *(const bf16x8*)(ws + OFF_BF + (((size_t)(4 * w + hh) * 28 + v) * 14 + ks) * 1024 + lane * 16);
  if (tid < 32) {
    const int r = tid >> 1, hf = tid & 1;
    u32x4 m = *(const u32x4*)(mem0 + (size_t)(r & 7) * DM + v * 16 + hf * 8);
    st16(rs, (unsigned)OFF_MEM + (v * 128 + tid * 4) * 4, ~m);
  }
  bf16x8 Wx[2][4];
  auto compute_wx = [&](const char* lx, int l, bf16x8 (&out)[4]) {
    f32x4 a0[4], a1[4];
#pragma unroll
    for (int hh = 0; hh < 4; ++hh) { a0[hh] = (f32x4){0.f, 0.f, 0.f, 0.f}; a1[hh] = a0[hh]; }
#pragma unroll
    for (int ks = 0; ks < 14; ++ks) {
      const bf16x8 x0 = *(const bf16x8*)(lx + (32 * l + n) * RS + (32 * ks + 8 * g) * 2);
      const bf16x8 x1 = *(const bf16x8*)(lx + (32 * l + 16 + n) * RS + (32 * ks + 8 * g) * 2);
#pragma unroll
      for (int hh = 0; hh < 4; ++hh) { a0[hh] = mfma32(x0, W[hh][ks], a0[hh]); a1[hh] = mfma32(x1, W[hh][ks], a1[hh]); }
    }
#pragma unroll
    for (int hh = 0; hh < 4; ++hh) out[hh] = pack8(a0[hh], a1[hh]);
  };
  load_xs(rs, ws, lxs0, 0, tid, 256);
  __syncthreads();
  compute_wx(lxs0, 0, Wx[0]);
  compute_wx(lxs0, 1, Wx[1]);
  if (NTICK > 1) load_xs(rs, ws, lxs0 + 64 * RS, 1, tid, 256);
  for (int tick = 0; tick < NTICK; ++tick) {
    const char* lxn = lxs0 + ((tick + 1) & 1) * 64 * RS;
    gather_mem(rs, (unsigned)OFF_MEM + tick * MEMW * 4, lmem, tid, ws);
    __syncthreads();
    bf16x4 Wm[4];
    {
      f32x4 a[4];
#pragma unroll
      for (int hh = 0; hh < 4; ++hh) a[hh] = (f32x4){0.f, 0.f, 0.f, 0.f};
#pragma unroll
      for (int ks = 0; ks < 14; ++ks) {
        const bf16x8 mf = *(const bf16x8*)(lmem + n * RS + (32 * ks + 8 * g) * 2);
#pragma unroll
        for (int hh = 0; hh < 4; ++hh) a[hh] = mfma32(mf, W[hh][ks], a[hh]);
      }
#pragma unroll
      for (int hh = 0; hh < 4; ++hh) Wm[hh] = pack4(a[hh]);
    }
    bf16x8 Wn[2][4];
    if (tick + 1 < NTICK) compute_wx(lxn, 0, Wn[0]);
    u32x2 pm[4]; u32x4 px[4];
    {
      const unsigned pb = (unsigned)OFF_PB + (tick * NH + 4 * w) * 384 * 4;
      bool all;
      SPIN_DECL;
      do {
        fence_c();
#pragma unroll
        for (int hh = 0; hh < 4; ++hh) { pm[hh] = ld8(rs, pb + hh * 1536 + lane * 8); px[hh] = ld16(rs, pb + hh * 1536 + 512 + lane * 16); }
        all = true;
#pragma unroll
        for (int hh = 0; hh < 4; ++hh) { all = all && pm[hh][0] && pm[hh][1] && nz4(px[hh]); }
        SPIN_CHK(ws, 3);
      } while (!all);
    }
    f32x4 o = {0.f, 0.f, 0.f, 0.f};
    const u32x4 z4 = {0u, 0u, 0u, 0u};
#pragma unroll
    for (int hh = 0; hh < 4; ++hh) {
      o = mfma16(__builtin_bit_cast(bf16x4, ~pm[hh]), Wm[hh], o);
      const u32x4 pp = ~px[hh];
      o = mfma32(__builtin_bit_cast(bf16x8, n < 8 ? pp : z4), Wx[0][hh], o);
      o = mfma32(__builtin_bit_cast(bf16x8, n < 8 ? z4 : pp), Wx[1][hh], o);
    }
#pragma unroll
    for (int i = 0; i < 4; ++i) red[(w * 16 + 4 * g + i) * 16 + n] = o[i];
    __syncthreads();
    if (tid < 32) {
      const int r = tid >> 1, hf = tid & 1;
      f32x4 s0 = {0.f, 0.f, 0.f, 0.f}, s1 = s0;
#pragma unroll
      for (int ww = 0; ww < 4; ++ww) { s0 += *(const f32x4*)(red + (ww * 16 + r) * 16 + hf * 8); s1 += *(const f32x4*)(red + (ww * 16 + r) * 16 + hf * 8 + 4); }
      u32x4 m = {pk2(s0[0], s0[1]), pk2(s0[2], s0[3]), pk2(s1[0], s1[1]), pk2(s1[2], s1[3])};
      if (r >= 8 && tick + 1 <= LAG) m = *(const u32x4*)(mem0 + (size_t)(r & 7) * DM + v * 16 + hf * 8);
      st16(rs, (unsigned)OFF_MEM + ((tick + 1) * MEMW + v * 128 + tid * 4) * 4, ~m);
    }
    if (tick + 1 < NTICK) {
      compute_wx(lxn, 1, Wn[1]);
#pragma unroll
      for (int hh = 0; hh < 4; ++hh) { Wx[0][hh] = Wn[0][hh]; Wx[1][hh] = Wn[1][hh]; }
    }
    __syncthreads();
    if (tick + 2 < NTICK) load_xs(rs, ws, lxs0 + (tick & 1) * 64 * RS, tick + 2, tid, 256);
  }
}

DI void role_W(char* ws, char* smem, int team, int h, unsigned short* out) {
  const int tid = threadIdx.x, lane = tid & 63, w = __builtin_amdgcn_readfirstlane(tid >> 6);
  const int n = lane & 15, g = lane >> 4;
  char* lh = smem;
  float* red = (float*)(smem + 48 * RS);
  char* lp = (char*)(red + 4 * 32 * 48);
  float* lst = (float*)(lp + 32 * 112);
  unsigned* cnt = (unsigned*)(ws + OFF_CNT);
  const rsrc_t rs = make_rsrc(ws);
  for (int i = tid; i < 8 * 57; i += 256) *(u32x4*)(lh + 40 * RS + i * 16) = (u32x4){0u, 0u, 0u, 0u};
  int ti = 0;
  for (int tau = 0; tau < NTICK; ++tau) {
    for (int l = 0; l < 2; ++l) {
      const int s = l == 0 ? tau : tau - LAG;
      if (s < 0 || s >= NSEG) continue;
      const int my = (ti % NTEAM) == team;
      ++ti;
      if (!my) continue;
      const int task = l * 64 + s;
      __syncthreads();
      if (l == 1) wait_cnt(cnt + (128 + s) * 32, 8, ws);
      {
        fence_c();
        const unsigned src = (unsigned)(l == 0 ? OFF_XS1 : OFF_XS2) + s * 32 * DM * 2;
        u32x4 v[7];
#pragma unroll
        for (int r = 0; r < 7; ++r) { const int c = tid + 256 * r; v[r] = ld16(rs, src + (c / 56) * DM * 2 + (c % 56) * 16); }
#pragma unroll
        for (int r = 0; r < 7; ++r) { const int c = tid + 256 * r; *(u32x4*)(lh + (c / 56) * RS + (c % 56) * 16) = v[r]; }
      }
      __syncthreads();
      bf16x8 xf[2][14];
#pragma unroll
      for (int mt = 0; mt < 2; ++mt)
#pragma unroll
        for (int ks = 0; ks < 14; ++ks) xf[mt][ks] = *(const bf16x8*)(lh + (16 * mt + n) * RS + (32 * ks + 8 * g) * 2);
      bf16x8 tA[2][3]; bf16x4 tB[2];
      {
        const char* Afs = ws + OFF_AF + (size_t)((h * 28 + 7 * w) * 14) * 1024;
        f32x4 prev[2];
        bf16x8 rb[3][7];
        ldw7(rb[0], lane * 16, lane * 16 + 4096, Afs);
        ldw7(rb[1], lane * 16, lane * 16 + 4096, Afs + 7 * 1024);
        f32x4 a0, a1;
#pragma unroll
        for (int u = 0; u < 14; ++u) {
          if (u + 2 < 14) ldw7(rb[(u + 2) % 3], lane * 16, lane * 16 + 4096, Afs + (u + 2) * 7 * 1024);
          if (u < 12) waitw7<14>(rb[u % 3]); else if (u == 12) waitw7<7>(rb[u % 3]); else waitw7<0>(rb[u % 3]);
          const int c = u >> 1, hf = u & 1;
          if (hf == 0) { a0 = (f32x4){0.f, 0.f, 0.f, 0.f}; a1 = a0; }
#pragma unroll
          for (int k = 0; k < 7; ++k) { a0 = mfma32(rb[u % 3][k], xf[0][7 * hf + k], a0); a1 = mfma32(rb[u % 3][k], xf[1][7 * hf + k], a1); }
          if (hf == 1) {
            if (c == 6) { tB[0] = pack4(a0); tB[1] = pack4(a1); }
            else if (c & 1) { tA[0][c >> 1] = pack8(prev[0], a0); tA[1][c >> 1] = pack8(prev[1], a1); }
            else { prev[0] = a0; prev[1] = a1; }
          }
        }
      }
      {
        const unsigned slot = (unsigned)OFF_MEM + (l == 0 ? s : s + LAG) * MEMW * 4;
        u32x4 v[2]; unsigned p[2];
#pragma unroll
        for (int r = 0; r < 2; ++r) { int c = tid + 256 * r; if (c >= 448) c = tid; p[r] = slot + ((c >> 4) * 128 + l * 64 + (c & 15) * 4) * 4; }
        bool all;
        SPIN_DECL;
        do {
          fence_c();
#pragma unroll
          for (int r = 0; r < 2; ++r) v[r] = ld16(rs, p[r]);
          all = true;
#pragma unroll
          for (int r = 0; r < 2; ++r) all = all && nz4(v[r]);
          if (!all) __builtin_amdgcn_s_sleep(4);
          SPIN_CHK(ws, 4);
        } while (!all);
#pragma unroll
        for (int r = 0; r < 2; ++r) {
          const int c = tid + 256 * r;
          if (c < 448) { const int vv = c >> 4, row = (c & 15) >> 1, hf = c & 1; *(u32x4*)(lh + (32 + row) * RS + (vv * 16 + hf * 8) * 2) = ~v[r]; }
        }
      }
      __syncthreads();
#pragma unroll
      for (int mt = 0; mt < 2; ++mt)
#pragma unroll
        for (int nt = 0; nt < 3; ++nt) {
          const char* rowp = lh + (16 * nt + n) * RS + (112 * w + 4 * g) * 2;
          f32x4 sacc = {0.f, 0.f, 0.f, 0.f};
#pragma unroll
          for (int p = 0; p < 3; ++p) {
            u32x2 lo = *(const u32x2*)(rowp + 64 * p), hi = *(const u32x2*)(rowp + 64 * p + 32);
            u32x4 kb = {lo[0], lo[1], hi[0], hi[1]};
            sacc = mfma32(tA[mt][p], __builtin_bit_cast(bf16x8, kb), sacc);
          }
          u32x2 lo = *(const u32x2*)(rowp + 192);
          sacc = mfma16(tB[mt], __builtin_bit_cast(bf16x4, lo), sacc);
#pragma unroll
          for (int i = 0; i < 4; ++i) red[(w * 32 + 16 * mt + 4 * g + i) * 48 + 16 * nt + n] = sacc[i];
        }
      __syncthreads();
      {
        const int r = tid >> 3, q = tid & 7;
        float sv[5]; float mx = -3.0e38f;
#pragma unroll
        for (int i = 0; i < 5; ++i) {
          const int k = q * 5 + i;
          float a = red[(0 * 32 + r) * 48 + k] + red[(1 * 32 + r) * 48 + k] + red[(2 * 32 + r) * 48 + k] + red[(3 * 32 + r) * 48 + k];
          a *= 0.0625f;
          const bool ok = (k >= 32) || (k <= r);
          sv[i] = ok ? a : -3.0e38f;
          mx = fmaxf(mx, sv[i]);
        }
        mx = fmaxf(mx, __shfl_xor(mx, 1)); mx = fmaxf(mx, __shfl_xor(mx, 2)); mx = fmaxf(mx, __shfl_xor(mx, 4));
        float sum = 0.f;
#pragma unroll
        for (int i = 0; i < 5; ++i) { sv[i] = sv[i] > -1.0e38f ? __expf(sv[i] - mx) : 0.f; sum += sv[i]; }
        sum += __shfl_xor(sum, 1); sum += __shfl_xor(sum, 2); sum += __shfl_xor(sum, 4);
        const float inv = 1.f / sum;
#pragma unroll
        for (int i = 0; i < 5; ++i) *(unsigned short*)(lp + r * 112 + (q * 5 + i) * 2) = f2bf(sv[i] * inv);
        if (q == 0) { *(u32x4*)(lp + r * 112 + 80) = (u32x4){0u, 0u, 0u, 0u}; }
      }
      __syncthreads();
      {
        bf16x8 pf[2]; bf16x4 pmf[2];
#pragma unroll
        for (int mt = 0; mt < 2; ++mt) {
          const char* pr = lp + (16 * mt + n) * 112;
          u32x2 lo = *(const u32x2*)(pr + 8 * g), hi = *(const u32x2*)(pr + 32 + 8 * g);
          pf[mt] = __builtin_bit_cast(bf16x8, (u32x4){lo[0], lo[1], hi[0], hi[1]});
          pmf[mt] = __builtin_bit_cast(bf16x4, *(const u32x2*)(pr + 64 + 8 * g));
        }
        const char* Bfs = ws + OFF_BF + (size_t)((h * 28 + 7 * w) * 14) * 1024;
        const unsigned part = (unsigned)OFF_PART + (task * NH + h) * (unsigned)SZ_PART1;
        bf16x8 rb[3][7];
        ldw7(rb[0], lane * 16, lane * 16 + 4096, Bfs);
        ldw7(rb[1], lane * 16, lane * 16 + 4096, Bfs + 7 * 1024);
        f32x4 a0, a1, a2;
#pragma unroll
        for (int u = 0; u < 14; ++u) {
          if (u + 2 < 14) ldw7(rb[(u + 2) % 3], lane * 16, lane * 16 + 4096, Bfs + (u + 2) * 7 * 1024);
          if (u < 12) waitw7<14>(rb[u % 3]); else if (u == 12) waitw7<7>(rb[u % 3]); else waitw7<0>(rb[u % 3]);
          const int c = u >> 1, hf = u & 1;
          if (hf == 0) { a0 = (f32x4){0.f, 0.f, 0.f, 0.f}; a1 = a0; a2 = a0; }
#pragma unroll
          for (int k = 0; k < 7; ++k) {
            const bf16x8 mf = *(const bf16x8*)(lh + (32 + n) * RS + (32 * (7 * hf + k) + 8 * g) * 2);
            a0 = mfma32(xf[0][7 * hf + k], rb[u % 3][k], a0); a1 = mfma32(xf[1][7 * hf + k], rb[u % 3][k], a1); a2 = mfma32(mf, rb[u % 3][k], a2);
          }
          if (hf == 1) {
            const bf16x8 wx = pack8(a0, a1);
            const bf16x4 wm = pack4(a2);
#pragma unroll
            for (int mt = 0; mt < 2; ++mt) {
              f32x4 o = {0.f, 0.f, 0.f, 0.f};
              o = mfma32(pf[mt], wx, o);
              o = mfma16(pmf[mt], wm, o);
              st16(rs, part + (((7 * w + c) * 2 + mt) * 64 + lane) * 16, __builtin_bit_cast(u32x4, o));
            }
          }
        }
      }
      wait_vm0();
      __syncthreads();
      if (tid == 0) add_cnt(cnt + task * 32);
      if (h < 8) {
        wait_cnt(cnt + task * 32, 16, ws);
        fence_c();
        const int mt = h >> 2, gq = h & 3, row0 = 16 * mt + 4 * gq;
        f32x4 a[2];
#pragma unroll
        for (int j = 0; j < 2; ++j) {
          a[j] = (f32x4){0.f, 0.f, 0.f, 0.f};
          int col = tid + 256 * j; if (col >= DM) col = tid;
          const unsigned p = (unsigned)OFF_PART + task * NH * (unsigned)SZ_PART1 + ((((col >> 4) * 2 + mt) * 64) + gq * 16 + (col & 15)) * 16;
          u32x4 v[16];
#pragma unroll
          for (int hh = 0; hh < 16; ++hh) v[hh] = ld16(rs, p + hh * (unsigned)SZ_PART1);
#pragma unroll
          for (int hh = 0; hh < 16; ++hh) a[j] += __builtin_bit_cast(f32x4, v[hh]);
        }
        const bool has2 = tid + 256 < DM;
        const size_t rbase = (size_t)(32 * s + row0) * DM;
        const unsigned xres = (unsigned)(l == 0 ? OFF_X1 : OFF_X2) + (unsigned)rbase * 2;
        float xv[2][4];
#pragma unroll
        for (int j = 0; j < 2; ++j) {
          const int col = tid + 256 * j;
#pragma unroll
          for (int i = 0; i < 4; ++i) {
            float xr = 0.f;
            if (j == 0 || has2) {
              xr = bf2f(ld2(rs, xres + (i * DM + col) * 2));
            }
            xv[j][i] = rbf(xr + rbf(a[j][i]));
          }
        }
        if (l == 1) {
#pragma unroll
          for (int j = 0; j < 2; ++j) {
            const int col = tid + 256 * j;
            if (j == 0 || has2)
#pragma unroll
              for (int i = 0; i < 4; ++i) out[rbase + i * DM + col] = f2bf(xv[j][i]);
          }
        } else {
          const unsigned short* y = (const unsigned short*)(ws + OFF_Y) + rbase;
          const unsigned x2 = (unsigned)OFF_X2 + (unsigned)rbase * 2;
          const unsigned xs2 = (unsigned)OFF_XS2 + (unsigned)rbase * 2;
#pragma unroll
          for (int pass = 0; pass < 2; ++pass) {
            float mean[4], var[4];
            {
              float sres[4];
#pragma unroll
              for (int i = 0; i < 4; ++i) sres[i] = wave_sum(xv[0][i] + (has2 ? xv[1][i] : 0.f));
              __syncthreads();
              if (lane == 0)
#pragma unroll
                for (int i = 0; i < 4; ++i) lst[w * 4 + i] = sres[i];
              __syncthreads();
#pragma unroll
              for (int i = 0; i < 4; ++i) mean[i] = (lst[i] + lst[4 + i] + lst[8 + i] + lst[12 + i]) * (1.f / DM);
#pragma unroll
              for (int i = 0; i < 4; ++i) {
                const float d0 = xv[0][i] - mean[i], d1 = xv[1][i] - mean[i];
                sres[i] = wave_sum(d0 * d0 + (has2 ? d1 * d1 : 0.f));
              }
              __syncthreads();
              if (lane == 0)
#pragma unroll
                for (int i = 0; i < 4; ++i) lst[w * 4 + i] = sres[i];
              __syncthreads();
#pragma unroll
              for (int i = 0; i < 4; ++i) var[i] = (lst[i] + lst[4 + i] + lst[8 + i] + lst[12 + i]) * (1.f / DM);
            }
#pragma unroll
            for (int i = 0; i < 4; ++i) {
              const float mb = rbf(mean[i]), den = rbf(sqrtf(rbf(rbf(var[i]) + 1e-5f)));
#pragma unroll
              for (int j = 0; j < 2; ++j) {
                const int col = tid + 256 * j;
                if (j == 0 || has2) {
                  const float ln = rbf(rbf(xv[j][i] - mb) / den);
                  if (pass == 0) {
                    xv[j][i] = rbf(xv[j][i] + rbf(ln * bf2f(y[i * DM + col])));
                    st2(rs, x2 + (i * DM + col) * 2, f2bf(xv[j][i]));
                  } else {
                    st2(rs, xs2 + (i * DM + col) * 2, f2bf(ln));
                  }
                }
              }
            }
          }
          wait_vm0();
          __syncthreads();
          if (tid == 0) add_cnt(cnt + (128 + s) * 32);
        }
      }
    }
  }
}

__global__ void __launch_bounds__(256, 1) k_main(char* __restrict__ ws, const unsigned short* __restrict__ mem0, unsigned short* __restrict__ out) {
  extern __shared__ __attribute__((aligned(16))) char smem[];
  const int b = blockIdx.x;
  if (b < 16) role_S(ws, smem, b);
  else if (b < 16 + NV) role_V(ws, smem, b - 16, mem0);
  else { const int k = b - 16 - NV; role_W(ws, smem, k >> 4, k & 15, out); }
}

extern "C" void kernel_launch(void* const* d_in, const int* in_sizes, int n_in, void* d_out, int out_size, void* d_ws,
                              size_t ws_size, hipStream_t stream) {
  const unsigned short* A_q = (const unsigned short*)d_in[0];
  const unsigned short* A_k = (const unsigned short*)d_in[1];
  const unsigned short* A_v = (const unsigned short*)d_in[2];
  const unsigned short* A_shift = (const unsigned short*)d_in[3];
  const unsigned short* A_ssmk = (const unsigned short*)d_in[4];
  const unsigned short* A_D = (const unsigned short*)d_in[5];
  const unsigned short* B_mem0 = (const unsigned short*)d_in[7];
  const unsigned short* B_Wq = (const unsigned short*)d_in[8];
  const unsigned short* B_Wk = (const unsigned short*)d_in[9];
  const unsigned short* B_Wv = (const unsigned short*)d_in[10];
  const unsigned short* B_Wo = (const unsigned short*)d_in[11];
  char* ws = (char*)d_ws;
  constexpr int LDS_PRE = (2 * 8 * HC_STRIDE + 2 * 4032) * 2 + (2048 + 8) * 4;
  constexpr int LDS_S = 80 * RS + 4 * 14 * 1024 + 4 * 5 * 16 * 16 * 4;
  constexpr int LDS_V = 144 * RS + 4 * 16 * 16 * 4;
  constexpr int LDS_MAIN = LDS_S > LDS_V ? LDS_S : LDS_V;
  hipFuncSetAttribute((const void*)k_pre, hipFuncAttributeMaxDynamicSharedMemorySize, LDS_PRE);
  hipFuncSetAttribute((const void*)k_main, hipFuncAttributeMaxDynamicSharedMemorySize, LDS_MAIN);
  hipMemsetAsync(ws + OFF_Z, 0, OFF_ZEND - OFF_Z, stream);
  k_pre<<<448, 256, LDS_PRE, stream>>>(A_k, A_v, A_shift, A_ssmk, A_D, B_Wq, B_Wk, B_Wv, B_Wo, ws);
  k_ln<<<SEQ / 4, 256, 0, stream>>>(A_q, ws);
  k_main<<<16 + NV + 16 * NTEAM, 256, LDS_MAIN, stream>>>(ws, B_mem0, (unsigned short*)d_out);
}
```

```cpp
#include <hip/hip_runtime.h>
#include <hip/hip_bf16.h>
#include <stdint.h>

typedef __attribute__((ext_vector_type(8))) short bf16x8;
typedef __attribute__((ext_vector_type(4))) short bf16x4;
typedef __attribute__((ext_vector_type(4))) float f32x4;
typedef __attribute__((ext_vector_type(16))) float f32x16;
typedef __attribute__((ext_vector_type(4))) unsigned u32x4;
typedef __attribute__((ext_vector_type(2))) unsigned u32x2;
typedef __attribute__((ext_vector_type(2))) float f32x2;
typedef __attribute__((ext_vector_type(2))) __bf16 bf2;
#define DI __device__ __forceinline__
#define GAS __attribute__((address_space(1)))

constexpr int DM = 448, SEQ = 2048, NH = 16, NSEG = 64;
#define LAGV 5
constexpr int LAG = LAGV;
constexpr int PRE_A = 4;
constexpr int BQ = 2;
constexpr int NTICK = NSEG + LAG;
constexpr int NTEAM = 13;
constexpr int NV = 28;
constexpr int RS = 912;
constexpr int MEMW = NV * 128;

constexpr size_t SZ_F = (size_t)NH * 28 * 14 * 1024;
constexpr size_t SZ_X = (size_t)SEQ * DM * 2;
constexpr size_t OFF_AF = 0;
constexpr size_t OFF_BF = OFF_AF + SZ_F;
constexpr size_t OFF_Y = OFF_BF + SZ_F;
constexpr size_t OFF_X1 = OFF_Y + SZ_X;
constexpr size_t OFF_XS1 = OFF_X1 + SZ_X;
constexpr size_t OFF_X2 = OFF_XS1 + SZ_X;
constexpr size_t OFF_XS2 = OFF_X2 + SZ_X;
constexpr size_t OFF_Z = OFF_XS2 + SZ_X;
constexpr size_t OFF_MEM = OFF_Z;
constexpr size_t SZ_MEM = (size_t)(NTICK + 1) * MEMW * 4;
constexpr size_t OFF_PB = OFF_MEM + SZ_MEM;
constexpr size_t SZ_PB = (size_t)NTICK * NH * 384 * 4;
constexpr size_t OFF_CNT = OFF_PB + SZ_PB;
constexpr int C_PART = 0, C_XS2 = 128, C_WX = 192, C_WM = 320, C_ABORT = 399;
constexpr size_t SZ_CNT = (size_t)400 * 128;
constexpr size_t OFF_ZEND = OFF_CNT + SZ_CNT;
constexpr size_t SZ_PART1 = (size_t)28 * 2 * 64 * 16;
constexpr size_t OFF_PART = (OFF_ZEND + 4095) / 4096 * 4096;
constexpr size_t OFF_WX = OFF_PART + (size_t)128 * NH * SZ_PART1;
constexpr size_t OFF_WM = OFF_WX + (size_t)128 * NH * 28 * 1024;
constexpr size_t OFF_END = OFF_WM + (size_t)(NTICK + 1) * NH * 28 * 512;
static_assert(OFF_END <= ((size_t)240 << 20), "workspace");

DI float bf2f(unsigned short u) { return __uint_as_float((unsigned)u << 16); }
DI unsigned short f2bf(float f) { __bf16 b = (__bf16)f; return __builtin_bit_cast(unsigned short, b); }
DI float rbf(float f) { return bf2f(f2bf(f)); }
DI unsigned pk2(float a, float b) { bf2 v = {(__bf16)a, (__bf16)b}; return __builtin_bit_cast(unsigned, v); }
DI f32x4 mfma32(bf16x8 a, bf16x8 b, f32x4 c) { return __builtin_amdgcn_mfma_f32_16x16x32_bf16(a, b, c, 0, 0, 0); }
DI f32x4 mfma16(bf16x4 a, bf16x4 b, f32x4 c) {
  const bf16x8 a8 = {a[0], a[1], a[2], a[3], 0, 0, 0, 0}, b8 = {b[0], b[1], b[2], b[3], 0, 0, 0, 0};
  return __builtin_amdgcn_mfma_f32_16x16x32_bf16(a8, b8, c, 0, 0, 0);
}
DI bf16x4 pack4(f32x4 v) { u32x2 p = {pk2(v[0], v[1]), pk2(v[2], v[3])}; return __builtin_bit_cast(bf16x4, p); }
DI bf16x8 pack8(f32x4 a, f32x4 b) { u32x4 p = {pk2(a[0], a[1]), pk2(a[2], a[3]), pk2(b[0], b[1]), pk2(b[2], b[3])}; return __builtin_bit_cast(bf16x8, p); }

typedef __amdgpu_buffer_rsrc_t rsrc_t;
constexpr int AUX_SC1 = 16;
DI rsrc_t make_rsrc(const void* p) { return __builtin_amdgcn_make_buffer_rsrc((void*)p, (short)0, (int)0x10000000, 0x00020000); }
DI u32x4 ld16(rsrc_t r, unsigned off) { return __builtin_amdgcn_raw_buffer_load_b128(r, off, 0, AUX_SC1); }
DI u32x2 ld8(rsrc_t r, unsigned off) { return __builtin_amdgcn_raw_buffer_load_b64(r, off, 0, AUX_SC1); }
DI unsigned ld4(rsrc_t r, unsigned off) { return __builtin_amdgcn_raw_buffer_load_b32(r, off, 0, AUX_SC1); }
DI void st16(rsrc_t r, unsigned off, u32x4 v) { __builtin_amdgcn_raw_buffer_store_b128(v, r, off, 0, AUX_SC1); }
DI void st8(rsrc_t r, unsigned off, u32x2 v) { __builtin_amdgcn_raw_buffer_store_b64(v, r, off, 0, AUX_SC1); }
DI void st4(rsrc_t r, unsigned off, unsigned v) { __builtin_amdgcn_raw_buffer_store_b32(v, r, off, 0, AUX_SC1); }
DI void SB() { __builtin_amdgcn_sched_barrier(0); }
DI void dma16(const void* gp, unsigned lds_off) {
  asm volatile("s_mov_b32 m0, %1\n s_nop 0\n global_load_lds_dwordx4 %0, off sc1" ::"v"(gp), "s"(lds_off) : "memory");
}
DI unsigned lds_off(const void* p) { return (unsigned)(size_t)(const __attribute__((address_space(3))) void*)p; }
DI void lda(bf16x8& d, const void* p) { asm volatile("global_load_dwordx4 %0, %1, off" : "=a"(d) : "v"(p)); }
DI void ldw7(bf16x8 (&d)[7], unsigned v0, unsigned v1, const char* base) {
  asm volatile(
      "s_nop 4\n"
      "global_load_dwordx4 %0, %7, %9\n"
      "global_load_dwordx4 %1, %7, %9 offset:1024\n"
      "global_load_dwordx4 %2, %7, %9 offset:2048\n"
      "global_load_dwordx4 %3, %7, %9 offset:3072\n"
      "global_load_dwordx4 %4, %8, %9\n"
      "global_load_dwordx4 %5, %8, %9 offset:1024\n"
      "global_load_dwordx4 %6, %8, %9 offset:2048\n"
      : "=&v"(d[0]), "=&v"(d[1]), "=&v"(d[2]), "=&v"(d[3]), "=&v"(d[4]), "=&v"(d[5]), "=&v"(d[6])
      : "v"(v0), "v"(v1), "s"(base));
}
template <int N> DI void waitw7(bf16x8 (&d)[7]) {
  asm volatile("s_waitcnt vmcnt(%7)" : "+v"(d[0]), "+v"(d[1]), "+v"(d[2]), "+v"(d[3]), "+v"(d[4]), "+v"(d[5]), "+v"(d[6]) : "n"(N));
}
DI void fence_c() { asm volatile("" ::: "memory"); }
DI void wait_vm0() { asm volatile("s_waitcnt vmcnt(0)" ::: "memory"); }
DI unsigned zc4(u32x4 v) { return (unsigned)(v[0] == 0u) | (unsigned)(v[1] == 0u) | (unsigned)(v[2] == 0u) | (unsigned)(v[3] == 0u); }
DI unsigned zc2(u32x2 v) { return (unsigned)(v[0] == 0u) | (unsigned)(v[1] == 0u); }
DI unsigned ld_cnt(const unsigned* p) { return __hip_atomic_load((const GAS unsigned*)p, __ATOMIC_RELAXED, __HIP_MEMORY_SCOPE_AGENT); }
DI void add_cnt(unsigned* p) { __hip_atomic_fetch_add((GAS unsigned*)p, 1u, __ATOMIC_RELAXED, __HIP_MEMORY_SCOPE_AGENT); }
#define SPIN_DECL
#define SPIN_CHK(ws, code)
#define TS(slot)
#define ST(var)
#define STS(slot, val)
DI void wait_cnt(const unsigned* p, unsigned target, const char* ws) {
  SPIN_DECL;
  while (ld_cnt(p) < target) { __builtin_amdgcn_s_sleep(8); SPIN_CHK((char*)ws, 1); }
}
DI void wait_cnt_wg(const unsigned* p, unsigned target, const char* ws) {
  if (threadIdx.x < 64) {
    SPIN_DECL;
    while (ld_cnt(p) < target) { __builtin_amdgcn_s_sleep(24); SPIN_CHK((char*)ws, 1); }
  }
  __syncthreads();
}

constexpr int HC_STRIDE = 2192;
__global__ void __launch_bounds__(256) k_pre(const unsigned short* __restrict__ gk, const unsigned short* __restrict__ gv,
                                             const unsigned short* __restrict__ gshift, const unsigned short* __restrict__ gssmk,
                                             const unsigned short* __restrict__ gD, const unsigned short* __restrict__ Wq,
                                             const unsigned short* __restrict__ Wk, const unsigned short* __restrict__ Wv,
                                             const unsigned short* __restrict__ Wo, char* __restrict__ ws) {
  extern __shared__ __attribute__((aligned(16))) char smem[];
  const int tid = threadIdx.x, lane = tid & 63, w = __builtin_amdgcn_readfirstlane(tid >> 6);
  if (blockIdx.x < 224) {
    const int d0 = blockIdx.x * 2;
    unsigned short* hc = (unsigned short*)smem;
    unsigned short* kvz = hc + 2 * 8 * HC_STRIDE;
    unsigned* kraw = (unsigned*)(kvz + 2 * 4032);
    if (tid < 4) kraw[tid] = 0;
    for (int t = tid; t < SEQ; t += 256) kraw[4 + t] = *(const unsigned*)(gk + (size_t)t * DM + d0);
    for (int i = tid; i < 2 * 1984; i += 256) { int ch = i / 1984; kvz[ch * 4032 + (i % 1984)] = 0; }
    for (int ch = 0; ch < 2; ++ch) {
      const unsigned short* hsrc = gssmk + (size_t)(d0 + ch) * SEQ;
      for (int i = tid; i < 8 * 2112; i += 256) {
        int q = i / 2112, u = i % 2112;
        int p = u + q;
        int idx = 2047 - p;
        hc[(ch * 8 + q) * HC_STRIDE + u] = (idx >= 0) ? hsrc[idx] : (unsigned short)0;
      }
    }
    __syncthreads();
    {
      float sh[2][4];
      for (int ch = 0; ch < 2; ++ch)
        for (int j = 0; j < 4; ++j) sh[ch][j] = bf2f(gshift[(d0 + ch) * 4 + j]);
      for (int t = tid; t < SEQ; t += 256) {
        unsigned vv = *(const unsigned*)(gv + (size_t)t * DM + d0);
#pragma unroll
        for (int ch = 0; ch < 2; ++ch) {
          float acc = 0.f;
#pragma unroll
          for (int j = 0; j < 4; ++j) {
            unsigned kk = kraw[4 + t - 3 + j];
            float kf = bf2f((unsigned short)(ch ? (kk >> 16) : (kk & 0xffff)));
            acc = rbf(acc + rbf(kf * sh[ch][3 - j]));
          }
          float vf = bf2f((unsigned short)(ch ? (vv >> 16) : (vv & 0xffff)));
          kvz[ch * 4032 + 1984 + t] = f2bf(acc * vf);
        }
      }
    }
    __syncthreads();
    {
      const int ch = w >> 1, mt = w & 1;
      const int n = lane & 31, hf = lane >> 5;
      const int b = 32 * mt + n;
      f32x16 acc;
#pragma unroll
      for (int i = 0; i < 16; ++i) acc[i] = 0.f;
      const unsigned short* hcc = hc + ch * 8 * HC_STRIDE;
      const unsigned short* kz = kvz + ch * 4032 + 1984;
      for (int c = 0; c < 32; ++c) {
#pragma unroll
        for (int ks = 0; ks < 4; ++ks) {
          const int j0 = 16 * ks + 8 * hf;
          const int p0 = 2047 - 64 * c - b + j0;
          const int q = p0 & 7, u = p0 - q;
          bf16x8 a = *(const bf16x8*)(hcc + q * HC_STRIDE + u);
          bf16x8 bb = *(const bf16x8*)(kz + 64 * (n - c) + j0);
          acc = __builtin_amdgcn_mfma_f32_32x32x16_bf16(a, bb, acc, 0, 0, 0);
        }
      }
      const float Dv = bf2f(gD[d0 + ch]);
      unsigned short* y = (unsigned short*)(ws + OFF_Y);
#pragma unroll
      for (int r = 0; r < 16; ++r) {
        const int row = (r & 3) + 8 * (r >> 2) + 4 * hf;
        const int t = 64 * n + 32 * mt + row;
        const float kvv = bf2f(kz[t]);
        const float yy = rbf(rbf(acc[r]) + rbf(kvv * Dv));
        y[(size_t)t * DM + d0 + ch] = f2bf(yy);
      }
    }
  } else {
    const int unit = (blockIdx.x - 224) * 4 + w;
    const int mat = unit / 448, rem = unit % 448, h = rem / 28, ct = rem % 28;
    const int n = lane & 15, g = lane >> 4;
    bf16x8 bfr[8];
    if (mat == 0) {
      const int wq = ct / 7, j = ct % 7;
      const int cc = j < 6 ? 112 * wq + 32 * (j >> 1) + 8 * (n >> 2) + 4 * (j & 1) + (n & 3) : 112 * wq + 96 + n;
      const unsigned short* p = Wk + ((size_t)cc * NH + h) * 256 + 8 * g;
#pragma unroll
      for (int es = 0; es < 8; ++es) bfr[es] = *(const bf16x8*)(p + 32 * es);
    } else {
      const unsigned short* p = Wo + ((size_t)h * 256 + 8 * g) * DM + 16 * ct + n;
#pragma unroll
      for (int es = 0; es < 8; ++es)
#pragma unroll
        for (int i = 0; i < 8; ++i) bfr[es][i] = (short)p[(size_t)(32 * es + i) * DM];
    }
    const unsigned short* Wa = mat == 0 ? Wq : Wv;
    unsigned short* dst = (unsigned short*)(ws + (mat == 0 ? OFF_AF : OFF_BF)) + ((size_t)(h * 28 + ct) * 14) * 512;
    for (int kt = 0; kt < 28; ++kt) {
      const unsigned short* pa = Wa + ((size_t)(16 * kt + n) * NH + h) * 256 + 8 * g;
      f32x4 acc = {0.f, 0.f, 0.f, 0.f};
#pragma unroll
      for (int es = 0; es < 8; ++es) acc = mfma32(*(const bf16x8*)(pa + 32 * es), bfr[es], acc);
      const int ks = kt >> 1, gp = 2 * (kt & 1) + (g >> 1), hf = g & 1;
      *(bf16x4*)(dst + (size_t)ks * 512 + (gp * 16 + n) * 8 + hf * 4) = pack4(acc);
    }
  }
}

DI float wave_sum(float v) {
#pragma unroll
  for (int o = 32; o > 0; o >>= 1) v += __shfl_xor(v, o);
  return v;
}

__global__ void __launch_bounds__(256) k_ln(const unsigned short* __restrict__ xin, char* __restrict__ ws) {
  const int row = blockIdx.x * 4 + (threadIdx.x >> 6), lane = threadIdx.x & 63;
  const unsigned short* y = (const unsigned short*)(ws + OFF_Y) + (size_t)row * DM;
  const unsigned short* x = xin + (size_t)row * DM;
  unsigned short* x1 = (unsigned short*)(ws + OFF_X1) + (size_t)row * DM;
  unsigned short* xs1 = (unsigned short*)(ws + OFF_XS1) + (size_t)row * DM;
  float v[7];
#pragma unroll
  for (int i = 0; i < 7; ++i) v[i] = bf2f(x[lane + 64 * i]);
#pragma unroll
  for (int pass = 0; pass < 2; ++pass) {
    float s = 0.f;
#pragma unroll
    for (int i = 0; i < 7; ++i) s += v[i];
    const float mean = wave_sum(s) * (1.f / DM);
    float q = 0.f;
#pragma unroll
    for (int i = 0; i < 7; ++i) q += (v[i] - mean) * (v[i] - mean);
    const float var = wave_sum(q) * (1.f / DM);
    const float mb = rbf(mean), den = rbf(sqrtf(rbf(rbf(var) + 1e-5f)));
    if (pass == 0) {
#pragma unroll
      for (int i = 0; i < 7; ++i) {
        const float ln = rbf(rbf(v[i] - mb) / den);
        v[i] = rbf(v[i] + rbf(ln * bf2f(y[lane + 64 * i])));
        x1[lane + 64 * i] = f2bf(v[i]);
      }
    } else {
#pragma unroll
      for (int i = 0; i < 7; ++i) xs1[lane + 64 * i] = f2bf(rbf(v[i] - mb) / den);
    }
  }
}

DI void gather_mem(rsrc_t rs, unsigned slot, char* lmem, int tid, char* ws) {
  u32x4 v[4];
  unsigned p[4];
#pragma unroll
  for (int r = 0; r < 4; ++r) { int c = tid + 256 * r; if (c >= 896) c = tid; p[r] = slot + c * 16; }
  bool all;
  SPIN_DECL;
  do {
    fence_c();
#pragma unroll
    for (int r = 0; r < 4; ++r) v[r] = ld16(rs, p[r]);
    unsigned bad = 0;
#pragma unroll
    for (int r = 0; r < 4; ++r) bad |= zc4(v[r]);
    all = bad == 0;
    SPIN_CHK(ws, 2);
  } while (!all);
#pragma unroll
  for (int r = 0; r < 4; ++r) {
    const int c = tid + 256 * r;
    if (c < 896) {
      const int vv = c >> 5, row = (c & 31) >> 1, hf = c & 1;
      *(u32x4*)(lmem + row * RS + (vv * 16 + hf * 8) * 2) = ~v[r];
    }
  }
}

DI void dma_xs(const char* ws, unsigned lxs_off, int tick, int wv, int nw, int lane) {
  const int s1 = tick < NSEG ? tick : 0;
  const int s2 = tick - LAG;
  const bool v2 = s2 >= 0;
  const char* src1 = ws + OFF_XS1 + (size_t)s1 * 32 * DM * 2;
  const char* src2 = v2 ? ws + OFF_XS2 + (size_t)s2 * 32 * DM * 2 : ws + OFF_XS1;
  if (v2) wait_cnt((const unsigned*)(ws + OFF_CNT) + (C_XS2 + s2) * 32, 8, ws);
  for (int j0 = wv; j0 < 57; j0 += nw) {
    const int j = j0 * 64 + lane;
    const int row = j / 57;
    int q = j - row * 57; if (q > 55) q = 55;
    dma16((row < 32 ? src1 + row * (DM * 2) : src2 + (row - 32) * (DM * 2)) + q * 16, lxs_off + j0 * 1024);
  }
}

DI void role_S(char* ws, char* smem, int h) {
  int tid = threadIdx.x;
  asm volatile("" : "+v"(tid));
  const int lane = tid & 63, w = __builtin_amdgcn_readfirstlane(tid >> 6);
  const int n = lane & 15, g = lane >> 4;
  char* lmem = smem;
  char* lxs = smem + 16 * RS;
  char* lw = lxs + 64 * RS;
  float* red = (float*)(lw + 4 * 14 * 1024);
  const rsrc_t rs = make_rsrc(ws);
  const char* Af = ws + OFF_AF + ((size_t)h * 28 + 7 * w) * 14 * 1024 + lane * 16;
  bf16x8 Wa[4][14], Wv[2][14];
#pragma unroll
  for (int c = 0; c < 4; ++c)
#pragma unroll
    for (int ks = 0; ks < 14; ++ks) lda(Wa[c][ks], Af + (c * 14 + ks) * 1024);
#pragma unroll
  for (int c = 0; c < 2; ++c)
#pragma unroll
    for (int ks = 0; ks < 14; ++ks) Wv[c][ks] = *(const bf16x8*)(Af + ((4 + c) * 14 + ks) * 1024);
#pragma unroll
  for (int ks = 0; ks < 14; ++ks) *(bf16x8*)(lw + (w * 14 + ks) * 1024 + lane * 16) = *(const bf16x8*)(Af + (6 * 14 + ks) * 1024);
  wait_vm0();
#pragma unroll
  for (int c = 0; c < 4; ++c)
#pragma unroll
    for (int ks = 0; ks < 14; ++ks) asm volatile("" : "+a"(Wa[c][ks]));
  const unsigned lxo = lds_off(lxs);
  dma_xs(ws, lxo, 0, w, 4, lane);
  const char* mrow = lmem + n * RS + 16 * g;
  const char* lwp = lw + w * 14 * 1024 + lane * 16;
  for (int tick = 0; tick < NTICK; ++tick) {
    gather_mem(rs, (unsigned)OFF_MEM + tick * MEMW * 4, lmem, tid, ws);
    wait_vm0();
    __syncthreads();
    TS((h * 80 + tick) * 4 + 0);
    ST(c0);
    f32x4 acc[7];
#pragma unroll
    for (int c = 0; c < 7; ++c) acc[c] = (f32x4){0.f, 0.f, 0.f, 0.f};
    bf16x8 mf[14], l0[7], l1[7], kf[5][3];
    bf16x4 kl[5];
#pragma unroll
    for (int ks = 0; ks < 14; ++ks) mf[ks] = *(const bf16x8*)(mrow + 64 * ks);
#pragma unroll
    for (int k = 0; k < 7; ++k) l0[k] = *(const bf16x8*)(lwp + k * 1024);
    SB();
#pragma unroll
    for (int ks = 0; ks < 14; ++ks) {
      if (ks == 2) {
#pragma unroll
        for (int k = 0; k < 7; ++k) l1[k] = *(const bf16x8*)(lwp + (7 + k) * 1024);
        SB();
      }
      if (ks == 10) {
#pragma unroll
        for (int nt = 0; nt < 5; ++nt) {
          const char* rowp = (nt == 0 ? lmem + n * RS : lxs + (16 * (nt - 1) + n) * RS) + 224 * w;
#pragma unroll
          for (int p = 0; p < 3; ++p) kf[nt][p] = *(const bf16x8*)(rowp + 64 * p + 16 * g);
          kl[nt] = *(const bf16x4*)(rowp + 192 + 8 * g);
        }
        SB();
      }
#pragma unroll
      for (int c = 0; c < 4; ++c) acc[c] = mfma32(Wa[c][ks], mf[ks], acc[c]);
#pragma unroll
      for (int c = 0; c < 2; ++c) acc[4 + c] = mfma32(Wv[c][ks], mf[ks], acc[4 + c]);
      acc[6] = mfma32(ks < 7 ? l0[ks] : l1[ks - 7], mf[ks], acc[6]);
      if (ks == 1 || ks == 9) SB();
    }
    ST(c1);
    bf16x8 tA[3];
#pragma unroll
    for (int p = 0; p < 3; ++p) tA[p] = pack8(acc[2 * p], acc[2 * p + 1]);
    const bf16x4 tB = pack4(acc[6]);
    f32x4 sc[5];
#pragma unroll
    for (int nt = 0; nt < 5; ++nt) sc[nt] = (f32x4){0.f, 0.f, 0.f, 0.f};
#pragma unroll
    for (int p = 0; p < 3; ++p)
#pragma unroll
      for (int nt = 0; nt < 5; ++nt) sc[nt] = mfma32(tA[p], kf[nt][p], sc[nt]);
#pragma unroll
    for (int nt = 0; nt < 5; ++nt) sc[nt] = mfma16(tB, kl[nt], sc[nt]);
    ST(c2);
#pragma unroll
    for (int nt = 0; nt < 5; ++nt)
#pragma unroll
      for (int i = 0; i < 4; ++i) red[((w * 5 + nt) * 16 + 4 * g + i) * 16 + n] = sc[nt][i];
    __syncthreads();
    ST(c3);
    if (w == 0) {
      const int r = n, l2 = r >> 3;
      f32x4 sm = {0.f, 0.f, 0.f, 0.f}, sa = sm, sb = sm;
#pragma unroll
      for (int ww = 0; ww < 4; ++ww) {
        sm += *(const f32x4*)(red + ((ww * 5 + 0) * 16 + r) * 16 + 4 * g);
        sa += *(const f32x4*)(red + ((ww * 5 + 1 + 2 * l2) * 16 + r) * 16 + 4 * g);
        sb += *(const f32x4*)(red + ((ww * 5 + 2 + 2 * l2) * 16 + r) * 16 + 4 * g);
      }
      const bool mv = (g >> 1) == l2;
      float mx = -3.0e38f;
#pragma unroll
      for (int i = 0; i < 4; ++i) { sm[i] *= 0.0625f; sa[i] *= 0.0625f; sb[i] *= 0.0625f; if (mv) mx = fmaxf(mx, sm[i]); mx = fmaxf(mx, fmaxf(sa[i], sb[i])); }
      mx = fmaxf(mx, __shfl_xor(mx, 16)); mx = fmaxf(mx, __shfl_xor(mx, 32));
      float sum = 0.f;
#pragma unroll
      for (int i = 0; i < 4; ++i) {
        sm[i] = mv ? __expf(sm[i] - mx) : 0.f; sa[i] = __expf(sa[i] - mx); sb[i] = __expf(sb[i] - mx);
        sum += sm[i] + sa[i] + sb[i];
      }
      sum += __shfl_xor(sum, 16); sum += __shfl_xor(sum, 32);
      const float inv = 1.f / sum;
      const unsigned pb = (unsigned)OFF_PB + (tick * NH + h) * 384 * 4;
      u32x2 pm = {~pk2(sm[0] * inv, sm[1] * inv), ~pk2(sm[2] * inv, sm[3] * inv)};
      u32x4 px = {~pk2(sa[0] * inv, sa[1] * inv), ~pk2(sa[2] * inv, sa[3] * inv), ~pk2(sb[0] * inv, sb[1] * inv), ~pk2(sb[2] * inv, sb[3] * inv)};
      st16(rs, pb + 512 + lane * 16, px);
      st8(rs, pb + lane * 8, pm);
      ST(c4);
      TS((h * 80 + tick) * 4 + 1);
      STS((h * 80 + tick) * 4 + 0, c1 - c0); STS((h * 80 + tick) * 4 + 1, c2 - c1); STS((h * 80 + tick) * 4 + 2, c3 - c2); STS((h * 80 + tick) * 4 + 3, c4 - c3);
    } else if (tick + 1 < NTICK) {
      dma_xs(ws, lxo, tick + 1, w - 1, 3, lane);
    }
  }
}

DI void role_V(char* ws, char* smem, int v, const unsigned short* mem0) {
  int tid = threadIdx.x;
  asm volatile("" : "+v"(tid));
  const int lane = tid & 63, w = __builtin_amdgcn_readfirstlane(tid >> 6);
  const int n = lane & 15, g = lane >> 4;
  char* lmem = smem;
  float* red = (float*)(smem + 16 * RS);
  const rsrc_t rs = make_rsrc(ws);
  unsigned* cnt = (unsigned*)(ws + OFF_CNT);
  bf16x8 W[4][14];
#pragma unroll
  for (int hh = 0; hh < 4; ++hh)
#pragma unroll
    for (int ks = 0; ks < 14; ++ks) lda(W[hh][ks], ws + OFF_BF + (((size_t)(4 * w + hh) * 28 + v) * 14 + ks) * 1024 + lane * 16);
  if (tid < 32) {
    const int r = tid >> 1, hf = tid & 1;
    u32x4 m = *(const u32x4*)(mem0 + (size_t)(r & 7) * DM + v * 16 + hf * 8);
    st16(rs, (unsigned)OFF_MEM + (v * 128 + tid * 4) * 4, ~m);
  }
  wait_vm0();
#pragma unroll
  for (int hh = 0; hh < 4; ++hh)
#pragma unroll
    for (int ks = 0; ks < 14; ++ks) asm volatile("" : "+a"(W[hh][ks]));
  u32x4 Wx[2][4];
  auto load_wx = [&](int tick) {
#pragma unroll
    for (int l = 0; l < 2; ++l) {
      const int sg = l == 0 ? tick : tick - LAG;
      if (sg >= 0 && sg < NSEG) {
        const int task = l * 64 + sg;
        wait_cnt(cnt + (C_WX + task) * 32, 16, ws);
        fence_c();
#pragma unroll
        for (int hh = 0; hh < 4; ++hh) Wx[l][hh] = ld16(rs, (unsigned)OFF_WX + ((task * NH + 4 * w + hh) * 28 + v) * 1024 + lane * 16);
      } else {
#pragma unroll
        for (int hh = 0; hh < 4; ++hh) Wx[l][hh] = (u32x4){0u, 0u, 0u, 0u};
      }
    }
  };
  load_wx(0);
  const char* mrow = lmem + n * RS + 16 * g;
  for (int tick = 0; tick < NTICK; ++tick) {
    gather_mem(rs, (unsigned)OFF_MEM + tick * MEMW * 4, lmem, tid, ws);
    __syncthreads();
    TS(((16 + v) * 80 + tick) * 4 + 0);
    ST(c0);
    bf16x4 Wm[4];
    {
      bf16x8 mf[14];
#pragma unroll
      for (int ks = 0; ks < 14; ++ks) mf[ks] = *(const bf16x8*)(mrow + 64 * ks);
      SB();
      f32x4 a[4];
#pragma unroll
      for (int hh = 0; hh < 4; ++hh) a[hh] = (f32x4){0.f, 0.f, 0.f, 0.f};
#pragma unroll
      for (int ks = 0; ks < 14; ++ks)
#pragma unroll
        for (int hh = 0; hh < 4; ++hh) a[hh] = mfma32(mf[ks], W[hh][ks], a[hh]);
#pragma unroll
      for (int hh = 0; hh < 4; ++hh) {
        Wm[hh] = pack4(a[hh]);
        st8(rs, (unsigned)OFF_WM + ((tick * NH + 4 * w + hh) * 28 + v) * 512 + lane * 8, __builtin_bit_cast(u32x2, Wm[hh]));
      }
    }
    ST(c1);
    u32x2 pm[4]; u32x4 px[4];
    {
      const unsigned pb = (unsigned)OFF_PB + (tick * NH + 4 * w) * 384 * 4;
      bool all;
      SPIN_DECL;
      do {
        fence_c();
#pragma unroll
        for (int hh = 0; hh < 4; ++hh) { pm[hh] = ld8(rs, pb + hh * 1536 + lane * 8); px[hh] = ld16(rs, pb + hh * 1536 + 512 + lane * 16); }
        unsigned bad = 0;
#pragma unroll
        for (int hh = 0; hh < 4; ++hh) bad |= zc2(pm[hh]) | zc4(px[hh]);
        all = bad == 0;
        SPIN_CHK(ws, 3);
      } while (!all);
    }
    TS(((16 + v) * 80 + tick) * 4 + 1);
    ST(c2);
    f32x4 o0 = {0.f, 0.f, 0.f, 0.f}, o1 = o0, o2 = o0;
    const u32x4 z4 = {0u, 0u, 0u, 0u};
#pragma unroll
    for (int hh = 0; hh < 4; ++hh) {
      o0 = mfma16(__builtin_bit_cast(bf16x4, ~pm[hh]), Wm[hh], o0);
      const u32x4 pp = ~px[hh];
      o1 = mfma32(__builtin_bit_cast(bf16x8, n < 8 ? pp : z4), __builtin_bit_cast(bf16x8, Wx[0][hh]), o1);
      o2 = mfma32(__builtin_bit_cast(bf16x8, n < 8 ? z4 : pp), __builtin_bit_cast(bf16x8, Wx[1][hh]), o2);
    }
    const f32x4 o = o0 + o1 + o2;
#pragma unroll
    for (int i = 0; i < 4; ++i) red[(w * 16 + 4 * g + i) * 16 + n] = o[i];
    ST(c3);
    wait_vm0();
    __syncthreads();
    ST(c4);
    if (tid < 32) {
      const int r = tid >> 1, hf = tid & 1;
      f32x4 s0 = {0.f, 0.f, 0.f, 0.f}, s1 = s0;
#pragma unroll
      for (int ww = 0; ww < 4; ++ww) { s0 += *(const f32x4*)(red + (ww * 16 + r) * 16 + hf * 8); s1 += *(const f32x4*)(red + (ww * 16 + r) * 16 + hf * 8 + 4); }
      u32x4 m = {pk2(s0[0], s0[1]), pk2(s0[2], s0[3]), pk2(s1[0], s1[1]), pk2(s1[2], s1[3])};
      if (r >= 8 && tick + 1 <= LAG) m = *(const u32x4*)(mem0 + (size_t)(r & 7) * DM + v * 16 + hf * 8);
      st16(rs, (unsigned)OFF_MEM + ((tick + 1) * MEMW + v * 128 + tid * 4) * 4, ~m);
    } else if (tid == 64) {
      add_cnt(cnt + (C_WM + tick) * 32);
    }
    ST(c5);
    TS(((16 + v) * 80 + tick) * 4 + 2);
    STS(((16 + v) * 80 + tick) * 4 + 0, c1 - c0); STS(((16 + v) * 80 + tick) * 4 + 1, c3 - c2); STS(((16 + v) * 80 + tick) * 4 + 2, c4 - c3); STS(((16 + v) * 80 + tick) * 4 + 3, c5 - c4);
    if (tick + 1 < NTICK) load_wx(tick + 1);
    TS(((16 + v) * 80 + tick) * 4 + 3);
  }
}

DI void load_x(rsrc_t rs, char* lh, unsigned src, int tid) {
  u32x4 v[7];
#pragma unroll
  for (int r = 0; r < 7; ++r) { const int c = tid + 256 * r; v[r] = ld16(rs, src + (c / 56) * DM * 2 + (c % 56) * 16); }
#pragma unroll
  for (int r = 0; r < 7; ++r) { const int c = tid + 256 * r; *(u32x4*)(lh + (c / 56) * RS + (c % 56) * 16) = v[r]; }
}

DI void role_W(char* ws, char* smem, int team, int h, unsigned short* out) {
  int tid = threadIdx.x;
  asm volatile("" : "+v"(tid));
  const int lane = tid & 63, w = __builtin_amdgcn_readfirstlane(tid >> 6);
  const int n = lane & 15, g = lane >> 4;
  char* lh = smem;
  float* red = (float*)(smem + 48 * RS);
  char* lp = (char*)(red + 4 * 32 * 48);
  float* lst = (float*)(lp + 32 * 112);
  unsigned* cnt = (unsigned*)(ws + OFF_CNT);
  const rsrc_t rs = make_rsrc(ws);
  for (int i = tid; i < 8 * 57; i += 256) *(u32x4*)(lh + 40 * RS + i * 16) = (u32x4){0u, 0u, 0u, 0u};
  int ti = 0;
  for (int key = -PRE_A; key < NTICK; ++key) {
    for (int un = 0; un < 4; ++un) {
      const int l = un & 1;
      const int s = un == 0 ? key + PRE_A : un == 1 ? key - BQ : un == 2 ? key : key - LAG;
      if (s < 0 || s >= NSEG) continue;
      const int my = (ti % NTEAM) == team;
      ++ti;
      if (!my) continue;
      const int task = l * 64 + s;
      __syncthreads();
      const int tsb = 44 * 80 * 4 + ((un >> 1) * 128 * 16 + task * 16 + h) * 8;
      TS(tsb + 0);
      if (l == 1) wait_cnt_wg(cnt + (C_XS2 + s) * 32, 8, ws);
      fence_c();
      load_x(rs, lh, (unsigned)(l == 0 ? OFF_XS1 : OFF_XS2) + s * 32 * DM * 2, tid);
      __syncthreads();
      bf16x8 xf[2][14];
#pragma unroll
      for (int mt = 0; mt < 2; ++mt)
#pragma unroll
        for (int ks = 0; ks < 14; ++ks) xf[mt][ks] = *(const bf16x8*)(lh + (16 * mt + n) * RS + (32 * ks + 8 * g) * 2);
      TS(tsb + 1);
      if (un < 2) {
        const char* Bfs = ws + OFF_BF + (size_t)((h * 28 + 7 * w) * 14) * 1024;
        const unsigned wxo = (unsigned)OFF_WX + ((task * NH + h) * 28 + 7 * w) * 1024 + lane * 16;
        bf16x8 rb[3][7];
        ldw7(rb[0], lane * 16, lane * 16 + 4096, Bfs);
        ldw7(rb[1], lane * 16, lane * 16 + 4096, Bfs + 7 * 1024);
        f32x4 a0, a1;
#pragma unroll
        for (int u = 0; u < 14; ++u) {
          if (u + 2 < 14) ldw7(rb[(u + 2) % 3], lane * 16, lane * 16 + 4096, Bfs + (u + 2) * 7 * 1024);
          if (u < 12) waitw7<14>(rb[u % 3]); else if (u == 12) waitw7<7>(rb[u % 3]); else waitw7<0>(rb[u % 3]);
          const int c = u >> 1, hf = u & 1;
          if (hf == 0) { a0 = (f32x4){0.f, 0.f, 0.f, 0.f}; a1 = a0; }
#pragma unroll
          for (int k = 0; k < 7; ++k) { a0 = mfma32(xf[0][7 * hf + k], rb[u % 3][k], a0); a1 = mfma32(xf[1][7 * hf + k], rb[u % 3][k], a1); }
          if (hf == 1) st16(rs, wxo + c * 1024, __builtin_bit_cast(u32x4, pack8(a0, a1)));
        }
        wait_vm0();
        __syncthreads();
        if (tid == 0) add_cnt(cnt + (C_WX + task) * 32);
        TS(tsb + 2);
        continue;
      }
      bf16x8 tA[2][3]; bf16x4 tB[2];
      {
        const char* Afs = ws + OFF_AF + (size_t)((h * 28 + 7 * w) * 14) * 1024;
        f32x4 prev[2];
        bf16x8 rb[3][7];
        ldw7(rb[0], lane * 16, lane * 16 + 4096, Afs);
        ldw7(rb[1], lane * 16, lane * 16 + 4096, Afs + 7 * 1024);
        f32x4 a0, a1;
#pragma unroll
        for (int u = 0; u < 14; ++u) {
          if (u + 2 < 14) ldw7(rb[(u + 2) % 3], lane * 16, lane * 16 + 4096, Afs + (u + 2) * 7 * 1024);
          if (u < 12) waitw7<14>(rb[u % 3]); else if (u == 12) waitw7<7>(rb[u % 3]); else waitw7<0>(rb[u % 3]);
          const int c = u >> 1, hf = u & 1;
          if (hf == 0) { a0 = (f32x4){0.f, 0.f, 0.f, 0.f}; a1 = a0; }
#pragma unroll
          for (int k = 0; k < 7; ++k) { a0 = mfma32(rb[u % 3][k], xf[0][7 * hf + k], a0); a1 = mfma32(rb[u % 3][k], xf[1][7 * hf + k], a1); }
          if (hf == 1) {
            if (c == 6) { tB[0] = pack4(a0); tB[1] = pack4(a1); }
            else if (c & 1) { tA[0][c >> 1] = pack8(prev[0], a0); tA[1][c >> 1] = pack8(prev[1], a1); }
            else { prev[0] = a0; prev[1] = a1; }
          }
        }
      }
      TS(tsb + 2);
      const int tk = l == 0 ? s : s + LAG;
      u32x4 wx[7]; u32x2 wm[7];
      wait_cnt_wg(cnt + (C_WX + task) * 32, 16, ws);
      fence_c();
#pragma unroll
      for (int c = 0; c < 7; ++c) wx[c] = ld16(rs, (unsigned)OFF_WX + ((task * NH + h) * 28 + 7 * w + c) * 1024 + lane * 16);
      wait_cnt_wg(cnt + (C_WM + tk) * 32, NV, ws);
      fence_c();
#pragma unroll
      for (int c = 0; c < 7; ++c) wm[c] = ld8(rs, (unsigned)OFF_WM + ((tk * NH + h) * 28 + 7 * w + c) * 512 + lane * 8);
      {
        const unsigned slot = (unsigned)OFF_MEM + tk * MEMW * 4;
        u32x4 v[2]; unsigned p[2];
#pragma unroll
        for (int r = 0; r < 2; ++r) { int c = tid + 256 * r; if (c >= 448) c = tid; p[r] = slot + ((c >> 4) * 128 + l * 64 + (c & 15) * 4) * 4; }
        bool all;
        SPIN_DECL;
        do {
          fence_c();
#pragma unroll
          for (int r = 0; r < 2; ++r) v[r] = ld16(rs, p[r]);
          all = (zc4(v[0]) | zc4(v[1])) == 0;
          if (!all) __builtin_amdgcn_s_sleep(4);
          SPIN_CHK(ws, 4);
        } while (!all);
#pragma unroll
        for (int r = 0; r < 2; ++r) {
          const int c = tid + 256 * r;
          if (c < 448) { const int vv = c >> 4, row = (c & 15) >> 1, hf = c & 1; *(u32x4*)(lh + (32 + row) * RS + (vv * 16 + hf * 8) * 2) = ~v[r]; }
        }
      }
      __syncthreads();
      TS(tsb + 3);
      {
        f32x4 sacc[2][3];
#pragma unroll
        for (int nt = 0; nt < 3; ++nt) {
          const char* rowp = lh + (16 * nt + n) * RS + 224 * w;
          bf16x8 kf[3];
#pragma unroll
          for (int p = 0; p < 3; ++p) kf[p] = *(const bf16x8*)(rowp + 64 * p + 16 * g);
          const bf16x4 kl = *(const bf16x4*)(rowp + 192 + 8 * g);
#pragma unroll
          for (int mt = 0; mt < 2; ++mt) {
            f32x4 sa = {0.f, 0.f, 0.f, 0.f};
#pragma unroll
            for (int p = 0; p < 3; ++p) sa = mfma32(tA[mt][p], kf[p], sa);
            sacc[mt][nt] = mfma16(tB[mt], kl, sa);
          }
        }
#pragma unroll
        for (int mt = 0; mt < 2; ++mt)
#pragma unroll
          for (int nt = 0; nt < 3; ++nt)
#pragma unroll
            for (int i = 0; i < 4; ++i) red[(w * 32 + 16 * mt + 4 * g + i) * 48 + 16 * nt + n] = sacc[mt][nt][i];
      }
      __syncthreads();
      {
        const int r = tid >> 3, q = tid & 7;
        float sv[5]; float mx = -3.0e38f;
#pragma unroll
        for (int i = 0; i < 5; ++i) {
          const int k = q * 5 + i;
          float a = red[(0 * 32 + r) * 48 + k] + red[(1 * 32 + r) * 48 + k] + red[(2 * 32 + r) * 48 + k] + red[(3 * 32 + r) * 48 + k];
          a *= 0.0625f;
          const bool ok = (k >= 32) || (k <= r);
          sv[i] = ok ? a : -3.0e38f;
          mx = fmaxf(mx, sv[i]);
        }
        mx = fmaxf(mx, __shfl_xor(mx, 1)); mx = fmaxf(mx, __shfl_xor(mx, 2)); mx = fmaxf(mx, __shfl_xor(mx, 4));
        float sum = 0.f;
#pragma unroll
        for (int i = 0; i < 5; ++i) { sv[i] = sv[i] > -1.0e38f ? __expf(sv[i] - mx) : 0.f; sum += sv[i]; }
        sum += __shfl_xor(sum, 1); sum += __shfl_xor(sum, 2); sum += __shfl_xor(sum, 4);
        const float inv = 1.f / sum;
#pragma unroll
        for (int i = 0; i < 5; ++i) { const int k = q * 5 + i; *(unsigned short*)(lp + r * 112 + (k < 32 ? k : k + 8 * l) * 2) = f2bf(sv[i] * inv); }
        if (q == 0) { *(u32x4*)(lp + r * 112 + 80 - 16 * l) = (u32x4){0u, 0u, 0u, 0u}; }
      }
      __syncthreads();
      TS(tsb + 4);
      {
        const unsigned part = (unsigned)OFF_PART + (task * NH + h) * (unsigned)SZ_PART1;
#pragma unroll
        for (int mt = 0; mt < 2; ++mt) {
          const char* pr = lp + (16 * mt + n) * 112;
          u32x2 lo = *(const u32x2*)(pr + 8 * g), hi = *(const u32x2*)(pr + 32 + 8 * g);
          const bf16x8 pf = __builtin_bit_cast(bf16x8, (u32x4){lo[0], lo[1], hi[0], hi[1]});
          const bf16x4 pmf = __builtin_bit_cast(bf16x4, *(const u32x2*)(pr + 64 + 8 * g));
#pragma unroll
          for (int c = 0; c < 7; ++c) {
            f32x4 o = {0.f, 0.f, 0.f, 0.f};
            o = mfma32(pf, __builtin_bit_cast(bf16x8, wx[c]), o);
            o = mfma16(pmf, __builtin_bit_cast(bf16x4, wm[c]), o);
            st16(rs, part + (((7 * w + c) * 2 + mt) * 64 + lane) * 16, __builtin_bit_cast(u32x4, o));
          }
        }
      }
      const int mt = (h >> 2) & 1, gq = h & 3, row0 = 16 * mt + 4 * gq;
      const bool act = tid < 224;
      const int col = act ? 2 * tid : 0;
      const unsigned rbase = (unsigned)((32 * s + row0) * DM + col) * 2;
      unsigned xr[4], yr[4];
      if (h < 8) {
#pragma unroll
        for (int i = 0; i < 4; ++i) {
          xr[i] = ld4(rs, (unsigned)(l == 0 ? OFF_X1 : OFF_X2) + rbase + i * DM * 2);
          yr[i] = l == 0 ? *(const unsigned*)(ws + OFF_Y + rbase + i * DM * 2) : 0u;
        }
      }
      wait_vm0();
      __syncthreads();
      if (tid == 0) add_cnt(cnt + (C_PART + task) * 32);
      TS(tsb + 5);
      if (h < 8) {
        wait_cnt_wg(cnt + (C_PART + task) * 32, 16, ws);
        fence_c();
        TS(tsb + 6);
        f32x4 a[2] = {{0.f, 0.f, 0.f, 0.f}, {0.f, 0.f, 0.f, 0.f}};
        const unsigned p = (unsigned)OFF_PART + task * NH * (unsigned)SZ_PART1 + ((((col >> 4) * 2 + mt) * 64) + gq * 16 + (col & 15)) * 16;
#pragma unroll
        for (int hb = 0; hb < 2; ++hb) {
          u32x4 v[8][2];
#pragma unroll
          for (int hh = 0; hh < 8; ++hh) { v[hh][0] = ld16(rs, p + (8 * hb + hh) * (unsigned)SZ_PART1); v[hh][1] = ld16(rs, p + (8 * hb + hh) * (unsigned)SZ_PART1 + 16); }
#pragma unroll
          for (int hh = 0; hh < 8; ++hh) { a[0] += __builtin_bit_cast(f32x4, v[hh][0]); a[1] += __builtin_bit_cast(f32x4, v[hh][1]); }
        }
        float xv[2][4];
#pragma unroll
        for (int i = 0; i < 4; ++i) {
          xv[0][i] = rbf(bf2f((unsigned short)(xr[i] & 0xffff)) + rbf(a[0][i]));
          xv[1][i] = rbf(bf2f((unsigned short)(xr[i] >> 16)) + rbf(a[1][i]));
        }
        if (l == 1) {
          if (act) {
#pragma unroll
            for (int i = 0; i < 4; ++i) *(unsigned*)((char*)out + rbase + i * DM * 2) = pk2(xv[0][i], xv[1][i]);
          }
        } else {
#pragma unroll
          for (int pass = 0; pass < 2; ++pass) {
            float mean[4], var[4], sres[4];
#pragma unroll
            for (int i = 0; i < 4; ++i) sres[i] = wave_sum(act ? xv[0][i] + xv[1][i] : 0.f);
            __syncthreads();
            if (lane == 0)
#pragma unroll
              for (int i = 0; i < 4; ++i) lst[w * 4 + i] = sres[i];
            __syncthreads();
#pragma unroll
            for (int i = 0; i < 4; ++i) mean[i] = (lst[i] + lst[4 + i] + lst[8 + i] + lst[12 + i]) * (1.f / DM);
#pragma unroll
            for (int i = 0; i < 4; ++i) {
              const float d0 = xv[0][i] - mean[i], d1 = xv[1][i] - mean[i];
              sres[i] = wave_sum(act ? d0 * d0 + d1 * d1 : 0.f);
            }
            __syncthreads();
            if (lane == 0)
#pragma unroll
              for (int i = 0; i < 4; ++i) lst[w * 4 + i] = sres[i];
            __syncthreads();
#pragma unroll
            for (int i = 0; i < 4; ++i) var[i] = (lst[i] + lst[4 + i] + lst[8 + i] + lst[12 + i]) * (1.f / DM);
#pragma unroll
            for (int i = 0; i < 4; ++i) {
              const float mb = rbf(mean[i]), den = rbf(sqrtf(rbf(rbf(var[i]) + 1e-5f)));
              const float ln0 = rbf(rbf(xv[0][i] - mb) / den), ln1 = rbf(rbf(xv[1][i] - mb) / den);
              if (pass == 0) {
                xv[0][i] = rbf(xv[0][i] + rbf(ln0 * bf2f((unsigned short)(yr[i] & 0xffff))));
                xv[1][i] = rbf(xv[1][i] + rbf(ln1 * bf2f((unsigned short)(yr[i] >> 16))));
                if (act) st4(rs, (unsigned)OFF_X2 + rbase + i * DM * 2, pk2(xv[0][i], xv[1][i]));
              } else {
                if (act) st4(rs, (unsigned)OFF_XS2 + rbase + i * DM * 2, pk2(ln0, ln1));
              }
            }
          }
          wait_vm0();
          __syncthreads();
          if (tid == 0) add_cnt(cnt + (C_XS2 + s) * 32);
        }
        TS(tsb + 7);
      }
    }
  }
}

__global__ void __launch_bounds__(256, 1) k_main(char* __restrict__ ws, const unsigned short* __restrict__ mem0, unsigned short* __restrict__ out) {
  extern __shared__ __attribute__((aligned(16))) char smem[];
  const int b = blockIdx.x;
  if (b < 16) role_S(ws, smem, b);
  else if (b < 16 + NV) role_V(ws, smem, b - 16, mem0);
  else { const int k = b - 16 - NV; role_W(ws, smem, k >> 4, k & 15, out); }
}

extern "C" void kernel_launch(void* const* d_in, const int* in_sizes, int n_in, void* d_out, int out_size, void* d_ws,
                              size_t ws_size, hipStream_t stream) {
  const unsigned short* A_q = (const unsigned short*)d_in[0];
  const unsigned short* A_k = (const unsigned short*)d_in[1];
  const unsigned short* A_v = (const unsigned short*)d_in[2];
  const unsigned short* A_shift = (const unsigned short*)d_in[3];
  const unsigned short* A_ssmk = (const unsigned short*)d_in[4];
  const unsigned short* A_D = (const unsigned short*)d_in[5];
  const unsigned short* B_mem0 = (const unsigned short*)d_in[7];
  const unsigned short* B_Wq = (const unsigned short*)d_in[8];
  const unsigned short* B_Wk = (const unsigned short*)d_in[9];
  const unsigned short* B_Wv = (const unsigned short*)d_in[10];
  const unsigned short* B_Wo = (const unsigned short*)d_in[11];
  char* ws = (char*)d_ws;
  constexpr int LDS_PRE = (2 * 8 * HC_STRIDE + 2 * 4032) * 2 + (2048 + 8) * 4;
  constexpr int LDS_S = 80 * RS + 4 * 14 * 1024 + 4 * 5 * 16 * 16 * 4;
  constexpr int LDS_V = 144 * RS + 4 * 16 * 16 * 4;
  constexpr int LDS_MAIN = LDS_S > LDS_V ? LDS_S : LDS_V;
  hipFuncSetAttribute((const void*)k_pre, hipFuncAttributeMaxDynamicSharedMemorySize, LDS_PRE);
  hipFuncSetAttribute((const void*)k_main, hipFuncAttributeMaxDynamicSharedMemorySize, LDS_MAIN);
  hipMemsetAsync(ws + OFF_Z, 0, OFF_ZEND - OFF_Z, stream);
  k_pre<<<448, 256, LDS_PRE, stream>>>(A_k, A_v, A_shift, A_ssmk, A_D, B_Wq, B_Wk, B_Wv, B_Wo, ws);
  k_ln<<<SEQ / 4, 256, 0, stream>>>(A_q, ws);
  k_main<<<16 + NV + 16 * NTEAM, 256, LDS_MAIN, stream>>>(ws, B_mem0, (unsigned short*)d_out);
}
```

```cpp
#include <hip/hip_runtime.h>
#include <hip/hip_bf16.h>
#include <stdint.h>

typedef __attribute__((ext_vector_type(8))) short bf16x8;
typedef __attribute__((ext_vector_type(4))) short bf16x4;
typedef __attribute__((ext_vector_type(4))) float f32x4;
typedef __attribute__((ext_vector_type(16))) float f32x16;
typedef __attribute__((ext_vector_type(4))) unsigned u32x4;
typedef __attribute__((ext_vector_type(2))) unsigned u32x2;
typedef __attribute__((ext_vector_type(2))) float f32x2;
typedef __attribute__((ext_vector_type(2))) __bf16 bf2;
#define DI __device__ __forceinline__
#define GAS __attribute__((address_space(1)))

constexpr int DM = 448, SEQ = 2048, NH = 16, NSEG = 64;
#define LAGV 5
constexpr int LAG = LAGV;
constexpr int PRE_A = 4;
#define BQV 2
constexpr int BQ = BQV;
constexpr int NTICK = NSEG + LAG;
constexpr int NTEAM = 13;
constexpr int NV = 28;
constexpr int RS = 912;
constexpr int MEMW = NV * 128;

constexpr size_t SZ_F = (size_t)NH * 28 * 14 * 1024;
constexpr size_t SZ_X = (size_t)SEQ * DM * 2;
constexpr size_t OFF_AF = 0;
constexpr size_t OFF_BF = OFF_AF + SZ_F;
constexpr size_t OFF_Y = OFF_BF + SZ_F;
constexpr size_t OFF_X1 = OFF_Y + SZ_X;
constexpr size_t OFF_XS1 = OFF_X1 + SZ_X;
constexpr size_t OFF_X2 = OFF_XS1 + SZ_X;
constexpr size_t OFF_XS2 = OFF_X2 + SZ_X;
constexpr size_t OFF_Z = OFF_XS2 + SZ_X;
constexpr size_t OFF_MEM = OFF_Z;
constexpr size_t SZ_MEM = (size_t)(NTICK + 1) * MEMW * 4;
constexpr size_t OFF_PB = OFF_MEM + SZ_MEM;
constexpr size_t SZ_PB = (size_t)NTICK * NH * 384 * 4;
constexpr size_t OFF_CNT = OFF_PB + SZ_PB;
constexpr int C_PART = 0, C_XS2 = 128, C_WX = 192, C_WM = 320, C_ABORT = 399;
constexpr size_t SZ_CNT = (size_t)400 * 128;
constexpr size_t OFF_ZEND = OFF_CNT + SZ_CNT;
constexpr size_t SZ_PART1 = (size_t)28 * 2 * 64 * 16;
constexpr size_t OFF_PART = (OFF_ZEND + 4095) / 4096 * 4096;
constexpr size_t OFF_WX = OFF_PART + (size_t)128 * NH * SZ_PART1;
constexpr size_t OFF_WM = OFF_WX + (size_t)128 * NH * 28 * 1024;
constexpr size_t OFF_END = OFF_WM + (size_t)(NTICK + 1) * NH * 28 * 512;
static_assert(OFF_END <= ((size_t)240 << 20), "workspace");
static_assert(OFF_Z % 16 == 0 && (OFF_ZEND - OFF_Z) % 16 == 0, "zero region");

DI float bf2f(unsigned short u) { return __uint_as_float((unsigned)u << 16); }
DI unsigned short f2bf(float f) { __bf16 b = (__bf16)f; return __builtin_bit_cast(unsigned short, b); }
DI float rbf(float f) { return bf2f(f2bf(f)); }
DI unsigned pk2(float a, float b) { bf2 v = {(__bf16)a, (__bf16)b}; return __builtin_bit_cast(unsigned, v); }
DI f32x4 mfma32(bf16x8 a, bf16x8 b, f32x4 c) { return __builtin_amdgcn_mfma_f32_16x16x32_bf16(a, b, c, 0, 0, 0); }
DI f32x4 mfma16(bf16x4 a, bf16x4 b, f32x4 c) {
  const bf16x8 a8 = {a[0], a[1], a[2], a[3], 0, 0, 0, 0}, b8 = {b[0], b[1], b[2], b[3], 0, 0, 0, 0};
  return __builtin_amdgcn_mfma_f32_16x16x32_bf16(a8, b8, c, 0, 0, 0);
}
DI bf16x4 pack4(f32x4 v) { u32x2 p = {pk2(v[0], v[1]), pk2(v[2], v[3])}; return __builtin_bit_cast(bf16x4, p); }
DI bf16x8 pack8(f32x4 a, f32x4 b) { u32x4 p = {pk2(a[0], a[1]), pk2(a[2], a[3]), pk2(b[0], b[1]), pk2(b[2], b[3])}; return __builtin_bit_cast(bf16x8, p); }

typedef __amdgpu_buffer_rsrc_t rsrc_t;
constexpr int AUX_SC1 = 16;
DI rsrc_t make_rsrc(const void* p) { return __builtin_amdgcn_make_buffer_rsrc((void*)p, (short)0, (int)0x10000000, 0x00020000); }
DI u32x4 ld16(rsrc_t r, unsigned off) { return __builtin_amdgcn_raw_buffer_load_b128(r, off, 0, AUX_SC1); }
DI u32x2 ld8(rsrc_t r, unsigned off) { return __builtin_amdgcn_raw_buffer_load_b64(r, off, 0, AUX_SC1); }
DI unsigned ld4(rsrc_t r, unsigned off) { return __builtin_amdgcn_raw_buffer_load_b32(r, off, 0, AUX_SC1); }
DI void st16(rsrc_t r, unsigned off, u32x4 v) { __builtin_amdgcn_raw_buffer_store_b128(v, r, off, 0, AUX_SC1); }
DI void st8(rsrc_t r, unsigned off, u32x2 v) { __builtin_amdgcn_raw_buffer_store_b64(v, r, off, 0, AUX_SC1); }
DI void st4(rsrc_t r, unsigned off, unsigned v) { __builtin_amdgcn_raw_buffer_store_b32(v, r, off, 0, AUX_SC1); }
DI void SB() { __builtin_amdgcn_sched_barrier(0); }
DI void dma16(const void* gp, unsigned lds_off) {
  asm volatile("s_mov_b32 m0, %1\n s_nop 0\n global_load_lds_dwordx4 %0, off sc1" ::"v"(gp), "s"(lds_off) : "memory");
}
DI unsigned lds_off(const void* p) { return (unsigned)(size_t)(const __attribute__((address_space(3))) void*)p; }
DI void lda(bf16x8& d, const void* p) { asm volatile("global_load_dwordx4 %0, %1, off" : "=a"(d) : "v"(p)); }
DI void ldw7(bf16x8 (&d)[7], unsigned v0, unsigned v1, const char* base) {
  asm volatile(
      "s_nop 4\n"
      "global_load_dwordx4 %0, %7, %9\n"
      "global_load_dwordx4 %1, %7, %9 offset:1024\n"
      "global_load_dwordx4 %2, %7, %9 offset:2048\n"
      "global_load_dwordx4 %3, %7, %9 offset:3072\n"
      "global_load_dwordx4 %4, %8, %9\n"
      "global_load_dwordx4 %5, %8, %9 offset:1024\n"
      "global_load_dwordx4 %6, %8, %9 offset:2048\n"
      : "=&v"(d[0]), "=&v"(d[1]), "=&v"(d[2]), "=&v"(d[3]), "=&v"(d[4]), "=&v"(d[5]), "=&v"(d[6])
      : "v"(v0), "v"(v1), "s"(base));
}
template <int N> DI void waitw7(bf16x8 (&d)[7]) {
  asm volatile("s_waitcnt vmcnt(%7)" : "+v"(d[0]), "+v"(d[1]), "+v"(d[2]), "+v"(d[3]), "+v"(d[4]), "+v"(d[5]), "+v"(d[6]) : "n"(N));
}
template <int N> DI float ror16(float v) { return __builtin_bit_cast(float, __builtin_amdgcn_update_dpp(0, __builtin_bit_cast(int, v), 0x120 + N, 0xf, 0xf, false)); }
DI void fence_c() { asm volatile("" ::: "memory"); }
DI void wait_vm0() { asm volatile("s_waitcnt vmcnt(0)" ::: "memory"); }
DI unsigned zc4(u32x4 v) { return (unsigned)(v[0] == 0u) | (unsigned)(v[1] == 0u) | (unsigned)(v[2] == 0u) | (unsigned)(v[3] == 0u); }
DI unsigned zc2(u32x2 v) { return (unsigned)(v[0] == 0u) | (unsigned)(v[1] == 0u); }
DI unsigned umin(unsigned a, unsigned b) { return a < b ? a : b; }
DI unsigned mn4(u32x4 v) { return umin(umin(v[0], v[1]), umin(v[2], v[3])); }
DI u32x4 not4(u32x4 v) { u32x4 r = ~v; asm volatile("" : "+v"(r)); return r; }
DI unsigned ld_cnt(const unsigned* p) { return __hip_atomic_load((const GAS unsigned*)p, __ATOMIC_RELAXED, __HIP_MEMORY_SCOPE_AGENT); }
DI void add_cnt(unsigned* p) { __hip_atomic_fetch_add((GAS unsigned*)p, 1u, __ATOMIC_RELAXED, __HIP_MEMORY_SCOPE_AGENT); }
#define SPIN_DECL
#define SPIN_CHK(ws, code)
#define TS(slot)
#define ST(var)
#define PTS(slot)
#define STS(slot, val)
DI void wait_cnt(const unsigned* p, unsigned target, const char* ws) {
  SPIN_DECL;
  while (ld_cnt(p) < target) { __builtin_amdgcn_s_sleep(8); SPIN_CHK((char*)ws, 1); }
}
DI void wait_cnt_wg(const unsigned* p, unsigned target, const char* ws) {
  if (threadIdx.x < 64) {
    SPIN_DECL;
    while (ld_cnt(p) < target) { __builtin_amdgcn_s_sleep(24); SPIN_CHK((char*)ws, 1); }
  }
  __syncthreads();
}

constexpr int HC_STRIDE = 2192;
constexpr int KV_CH = 63 * 72;
__global__ void __launch_bounds__(256) k_pre(const unsigned short* __restrict__ gk, const unsigned short* __restrict__ gv,
                                             const unsigned short* __restrict__ gshift, const unsigned short* __restrict__ gssmk,
                                             const unsigned short* __restrict__ gD, char* __restrict__ ws) {
  extern __shared__ __attribute__((aligned(16))) char smem[];
  const int tid = threadIdx.x, lane = tid & 63, w = __builtin_amdgcn_readfirstlane(tid >> 6);
  {
    const int d0 = blockIdx.x * 2;
    unsigned short* hc = (unsigned short*)smem;
    unsigned short* kvz = hc + 2 * 8 * HC_STRIDE;
    unsigned* kraw = (unsigned*)(kvz + 2 * KV_CH);
    PTS(0);
    for (size_t i = (size_t)blockIdx.x * 256 + tid; i < (OFF_ZEND - OFF_Z) / 16; i += 224 * 256) *(u32x4*)(ws + OFF_Z + i * 16) = (u32x4){0u, 0u, 0u, 0u};
    unsigned* kr0 = kraw;
    unsigned short* hr = (unsigned short*)(kraw + 2048 + 8);
    unsigned kk8[8], vv8[8];
#pragma unroll
    for (int i = 0; i < 8; ++i) kk8[i] = *(const unsigned*)(gk + (size_t)(tid + 256 * i) * DM + d0);
#pragma unroll
    for (int i = 0; i < 8; ++i) vv8[i] = *(const unsigned*)(gv + (size_t)(tid + 256 * i) * DM + d0);
    {
      const int ch = tid >> 7, i0 = (tid & 127) * 16;
      const u32x4 a = *(const u32x4*)(gssmk + (size_t)(d0 + ch) * SEQ + i0), b = *(const u32x4*)(gssmk + (size_t)(d0 + ch) * SEQ + i0 + 8);
      if (tid < 4) kr0[tid] = 0;
      for (int i = tid; i < 2 * 279; i += 256) *(u32x4*)(kvz + (i / 279) * KV_CH + (i % 279) * 8) = (u32x4){0u, 0u, 0u, 0u};
#pragma unroll
      for (int i = 0; i < 8; ++i) kr0[4 + tid + 256 * i] = kk8[i];
      auto sw = [](unsigned x) { return (x >> 16) | (x << 16); };
      *(u32x4*)(hr + ch * 2128 + 2040 - i0) = (u32x4){sw(a[3]), sw(a[2]), sw(a[1]), sw(a[0])};
      *(u32x4*)(hr + ch * 2128 + 2032 - i0) = (u32x4){sw(b[3]), sw(b[2]), sw(b[1]), sw(b[0])};
      if (tid < 20) *(u32x4*)(hr + (tid / 10) * 2128 + 2048 + (tid % 10) * 8) = (u32x4){0u, 0u, 0u, 0u};
    }
    __syncthreads();
    PTS(1);
    for (int it = tid; it < 2 * 264; it += 256) {
      const int ch = it / 264, u = (it % 264) * 8;
      const u32x4 a = *(const u32x4*)(hr + ch * 2128 + u), b = *(const u32x4*)(hr + ch * 2128 + u + 8);
      const unsigned d[8] = {a[0], a[1], a[2], a[3], b[0], b[1], b[2], b[3]};
#pragma unroll
      for (int q = 0; q < 8; ++q) {
        u32x4 o;
#pragma unroll
        for (int k = 0; k < 4; ++k) o[k] = (q & 1) ? __builtin_amdgcn_alignbyte(d[(q >> 1) + k + 1], d[(q >> 1) + k], 2) : d[(q >> 1) + k];
        *(u32x4*)(hc + (ch * 8 + q) * HC_STRIDE + u) = o;
      }
    }
    __syncthreads();
    PTS(2);
    {
      float sh[2][4];
      for (int ch = 0; ch < 2; ++ch)
        for (int j = 0; j < 4; ++j) sh[ch][j] = bf2f(gshift[(d0 + ch) * 4 + j]);
#pragma unroll
      for (int it = 0; it < 8; ++it) {
        const int t = tid + 256 * it;
        const unsigned vv = vv8[it];
#pragma unroll
        for (int ch = 0; ch < 2; ++ch) {
          float acc = 0.f;
#pragma unroll
          for (int j = 0; j < 4; ++j) {
            unsigned kk = kraw[4 + t - 3 + j];
            float kf = bf2f((unsigned short)(ch ? (kk >> 16) : (kk & 0xffff)));
            acc = rbf(acc + rbf(kf * sh[ch][3 - j]));
          }
          float vf = bf2f((unsigned short)(ch ? (vv >> 16) : (vv & 0xffff)));
          kvz[ch * KV_CH + (31 + (t >> 6)) * 72 + (t & 63)] = f2bf(acc * vf);
        }
      }
    }
    __syncthreads();
    PTS(3);
    {
      const int ch = w >> 1, mt = w & 1;
      const int n = lane & 31, hf = lane >> 5;
      const int b = 32 * mt + n;
      f32x16 acc;
#pragma unroll
      for (int i = 0; i < 16; ++i) acc[i] = 0.f;
      const unsigned short* hcc = hc + ch * 8 * HC_STRIDE;
      const unsigned short* kz = kvz + ch * KV_CH + 31 * 72;
      for (int c = 0; c < 32; ++c) {
#pragma unroll
        for (int ks = 0; ks < 4; ++ks) {
          const int j0 = 16 * ks + 8 * hf;
          const int p0 = 2047 - 64 * c - b + j0;
          const int q = p0 & 7, u = p0 - q;
          bf16x8 a = *(const bf16x8*)(hcc + q * HC_STRIDE + u);
          bf16x8 bb = *(const bf16x8*)(kz + 72 * (n - c) + j0);
          acc = __builtin_amdgcn_mfma_f32_32x32x16_bf16(a, bb, acc, 0, 0, 0);
        }
      }
      PTS(4);
      const float Dv = bf2f(gD[d0 + ch]);
      unsigned short* y = (unsigned short*)(ws + OFF_Y);
#pragma unroll
      for (int r = 0; r < 16; ++r) {
        const int row = (r & 3) + 8 * (r >> 2) + 4 * hf;
        const int t = 64 * n + 32 * mt + row;
        const float kvv = bf2f(kz[(t >> 6) * 72 + (t & 63)]);
        const float yy = rbf(rbf(acc[r]) + rbf(kvv * Dv));
        y[(size_t)t * DM + d0 + ch] = f2bf(yy);
      }
      PTS(5);
    }
  }
}

DI float wave_sum(float v) {
#pragma unroll
  for (int o = 32; o > 0; o >>= 1) v += __shfl_xor(v, o);
  return v;
}

DI void fold_unit(int unit, int lane, const unsigned short* __restrict__ Wq, const unsigned short* __restrict__ Wk,
                  const unsigned short* __restrict__ Wv, const unsigned short* __restrict__ Wo, char* __restrict__ ws) {
  const int kq = unit & 3, u4 = unit >> 2;
  const int mat = u4 / 112, rem = u4 % 112, h = rem / 7, cg = rem % 7;
  const int n = lane & 15, g = lane >> 4;
  const unsigned short* Wa = mat == 0 ? Wq : Wv;
  bf16x8 af[2][8];
  {
    const unsigned short* pa = Wa + ((size_t)(16 * 7 * kq + n) * NH + h) * 256 + 8 * g;
#pragma unroll
    for (int es = 0; es < 8; ++es) af[0][es] = *(const bf16x8*)(pa + 32 * es);
  }
  bf16x8 bfr[4][8];
#pragma unroll
  for (int c4 = 0; c4 < 4; ++c4) {
    const int ct = 4 * cg + c4;
    if (mat == 0) {
      const int wq = ct / 7, j = ct % 7;
      const int cc = j < 6 ? 112 * wq + 32 * (j >> 1) + 8 * (n >> 2) + 4 * (j & 1) + (n & 3) : 112 * wq + 96 + n;
      const unsigned short* p = Wk + ((size_t)cc * NH + h) * 256 + 8 * g;
#pragma unroll
      for (int es = 0; es < 8; ++es) bfr[c4][es] = *(const bf16x8*)(p + 32 * es);
    } else {
      const unsigned short* p = Wo + ((size_t)h * 256 + 8 * g) * DM + 16 * ct + n;
#pragma unroll
      for (int es = 0; es < 8; ++es)
#pragma unroll
        for (int i = 0; i < 8; ++i) bfr[c4][es][i] = (short)p[(size_t)(32 * es + i) * DM];
    }
  }
  unsigned short* dst = (unsigned short*)(ws + (mat == 0 ? OFF_AF : OFF_BF)) + ((size_t)(h * 28 + 4 * cg) * 14) * 512;
#pragma unroll
  for (int k7 = 0; k7 < 7; ++k7) {
    const int kt = 7 * kq + k7, cu = k7 & 1;
    if (k7 + 1 < 7) {
      const unsigned short* pa = Wa + ((size_t)(16 * (kt + 1) + n) * NH + h) * 256 + 8 * g;
#pragma unroll
      for (int es = 0; es < 8; ++es) af[cu ^ 1][es] = *(const bf16x8*)(pa + 32 * es);
    }
    const int ks = kt >> 1, gp = 2 * (kt & 1) + (g >> 1), hf = g & 1;
#pragma unroll
    for (int c4 = 0; c4 < 4; ++c4) {
      f32x4 acc = {0.f, 0.f, 0.f, 0.f};
#pragma unroll
      for (int es = 0; es < 8; ++es) acc = mfma32(af[cu][es], bfr[c4][es], acc);
      *(bf16x4*)(dst + (size_t)(c4 * 14 + ks) * 512 + (gp * 16 + n) * 8 + hf * 4) = pack4(acc);
    }
  }
}

__global__ void __launch_bounds__(256) k_ln(const unsigned short* __restrict__ xin, const unsigned short* __restrict__ Wq,
                                            const unsigned short* __restrict__ Wk, const unsigned short* __restrict__ Wv,
                                            const unsigned short* __restrict__ Wo, char* __restrict__ ws) {
  if (blockIdx.x >= SEQ / 4) { fold_unit((blockIdx.x - SEQ / 4) * 4 + (threadIdx.x >> 6), threadIdx.x & 63, Wq, Wk, Wv, Wo, ws); return; }
  const int row = blockIdx.x * 4 + (threadIdx.x >> 6), lane = threadIdx.x & 63;
  const unsigned short* y = (const unsigned short*)(ws + OFF_Y) + (size_t)row * DM;
  const unsigned short* x = xin + (size_t)row * DM;
  unsigned short* x1 = (unsigned short*)(ws + OFF_X1) + (size_t)row * DM;
  unsigned short* xs1 = (unsigned short*)(ws + OFF_XS1) + (size_t)row * DM;
  float v[7];
#pragma unroll
  for (int i = 0; i < 7; ++i) v[i] = bf2f(x[lane + 64 * i]);
#pragma unroll
  for (int pass = 0; pass < 2; ++pass) {
    float s = 0.f;
#pragma unroll
    for (int i = 0; i < 7; ++i) s += v[i];
    const float mean = wave_sum(s) * (1.f / DM);
    float q = 0.f;
#pragma unroll
    for (int i = 0; i < 7; ++i) q += (v[i] - mean) * (v[i] - mean);
    const float var = wave_sum(q) * (1.f / DM);
    const float mb = rbf(mean), den = rbf(sqrtf(rbf(rbf(var) + 1e-5f)));
    if (pass == 0) {
#pragma unroll
      for (int i = 0; i < 7; ++i) {
        const float ln = rbf(rbf(v[i] - mb) / den);
        v[i] = rbf(v[i] + rbf(ln * bf2f(y[lane + 64 * i])));
        x1[lane + 64 * i] = f2bf(v[i]);
      }
    } else {
#pragma unroll
      for (int i = 0; i < 7; ++i) xs1[lane + 64 * i] = f2bf(rbf(v[i] - mb) / den);
    }
  }
}

DI void gather_mem(rsrc_t rs, unsigned slot, char* lmem, int tid, char* ws) {
  u32x4 v[4];
  unsigned p[4];
#pragma unroll
  for (int r = 0; r < 4; ++r) { int c = tid + 256 * r; if (c >= 896) c = tid; p[r] = slot + c * 16; }
  bool all;
  SPIN_DECL;
  do {
    fence_c();
#pragma unroll
    for (int r = 0; r < 4; ++r) v[r] = ld16(rs, p[r]);
    all = umin(umin(mn4(v[0]), mn4(v[1])), umin(mn4(v[2]), mn4(v[3]))) != 0;
    SPIN_CHK(ws, 2);
  } while (!all);
#pragma unroll
  for (int r = 0; r < 4; ++r) {
    const int c = tid + 256 * r;
    if (c < 896) {
      const int vv = c >> 5, row = (c & 31) >> 1, hf = c & 1;
      *(u32x4*)(lmem + row * RS + (vv * 16 + hf * 8) * 2) = ~v[r];
    }
  }
}

DI void dma_xs(const char* ws, unsigned lxs_off, int tick, int wv, int nw, int lane, bool wait) {
  const int s1 = tick < NSEG ? tick : 0;
  const int s2 = tick - LAG;
  const bool v2 = s2 >= 0;
  const char* src1 = ws + OFF_XS1 + (size_t)s1 * 32 * DM * 2;
  const char* src2 = v2 ? ws + OFF_XS2 + (size_t)s2 * 32 * DM * 2 : ws + OFF_XS1;
  if (v2 && wait) wait_cnt((const unsigned*)(ws + OFF_CNT) + (C_XS2 + s2) * 32, 8, ws);
  for (int j0 = wv; j0 < 57; j0 += nw) {
    const int j = j0 * 64 + lane;
    const int row = j / 57;
    int q = j - row * 57; if (q > 55) q = 55;
    dma16((row < 32 ? src1 + row * (DM * 2) : src2 + (row - 32) * (DM * 2)) + q * 16, lxs_off + j0 * 1024);
  }
}

DI void role_S(char* ws, char* smem, int h) {
  int tid = threadIdx.x;
  asm volatile("" : "+v"(tid));
  const int lane = tid & 63, w = __builtin_amdgcn_readfirstlane(tid >> 6);
  const int n = lane & 15, g = lane >> 4;
  char* lmem = smem;
  char* lxs = smem + 16 * RS;
  char* lw = lxs + 64 * RS;
  float* red = (float*)(lw + 4 * 14 * 1024);
  const rsrc_t rs = make_rsrc(ws);
  const char* Af = ws + OFF_AF + ((size_t)h * 28 + 7 * w) * 14 * 1024 + lane * 16;
  bf16x8 Wa[4][14], Wv[2][14];
#pragma unroll
  for (int c = 0; c < 4; ++c)
#pragma unroll
    for (int ks = 0; ks < 14; ++ks) lda(Wa[c][ks], Af + (c * 14 + ks) * 1024);
#pragma unroll
  for (int c = 0; c < 2; ++c)
#pragma unroll
    for (int ks = 0; ks < 14; ++ks) Wv[c][ks] = *(const bf16x8*)(Af + ((4 + c) * 14 + ks) * 1024);
#pragma unroll
  for (int ks = 0; ks < 14; ++ks) *(bf16x8*)(lw + (w * 14 + ks) * 1024 + lane * 16) = *(const bf16x8*)(Af + (6 * 14 + ks) * 1024);
  wait_vm0();
#pragma unroll
  for (int c = 0; c < 4; ++c)
#pragma unroll
    for (int ks = 0; ks < 14; ++ks) asm volatile("" : "+a"(Wa[c][ks]));
  const unsigned lxo = lds_off(lxs);
  dma_xs(ws, lxo, 0, w, 4, lane, true);
  const char* mrow = lmem + n * RS + 16 * g;
  const char* lwp = lw + w * 14 * 1024 + lane * 16;
  for (int tick = 0; tick < NTICK; ++tick) {
    gather_mem(rs, (unsigned)OFF_MEM + tick * MEMW * 4, lmem, tid, ws);
    wait_vm0();
    __syncthreads();
    TS((h * 80 + tick) * 4 + 0);
    ST(c0);
    const int s2n = tick + 1 - LAG;
    const unsigned nxc = ld4(rs, (unsigned)OFF_CNT + (C_XS2 + (s2n >= 0 ? s2n : 0)) * 128);
    f32x4 acc[7];
#pragma unroll
    for (int c = 0; c < 7; ++c) acc[c] = (f32x4){0.f, 0.f, 0.f, 0.f};
    bf16x8 mf[14], l0[7], l1[7], kf[5][3];
    bf16x4 kl[5];
#pragma unroll
    for (int ks = 0; ks < 7; ++ks) { mf[ks] = *(const bf16x8*)(mrow + 64 * ks); l0[ks] = *(const bf16x8*)(lwp + ks * 1024); }
    SB();
#pragma unroll
    for (int ks = 0; ks < 14; ++ks) {
      if (ks == 1) {
#pragma unroll
        for (int k = 0; k < 7; ++k) { mf[7 + k] = *(const bf16x8*)(mrow + 64 * (7 + k)); l1[k] = *(const bf16x8*)(lwp + (7 + k) * 1024); }
        SB();
      }
      if (ks == 10) {
#pragma unroll
        for (int nt = 0; nt < 5; ++nt) {
          const char* rowp = (nt == 0 ? lmem + n * RS : lxs + (16 * (nt - 1) + n) * RS) + 224 * w;
#pragma unroll
          for (int p = 0; p < 3; ++p) kf[nt][p] = *(const bf16x8*)(rowp + 64 * p + 16 * g);
          kl[nt] = *(const bf16x4*)(rowp + 192 + 8 * g);
        }
        SB();
      }
#pragma unroll
      for (int c = 0; c < 4; ++c) acc[c] = mfma32(Wa[c][ks], mf[ks], acc[c]);
#pragma unroll
      for (int c = 0; c < 2; ++c) acc[4 + c] = mfma32(Wv[c][ks], mf[ks], acc[4 + c]);
      acc[6] = mfma32(ks < 7 ? l0[ks] : l1[ks - 7], mf[ks], acc[6]);
      if (ks == 0 || ks == 9) SB();
    }
    ST(c1);
    bf16x8 tA[3];
#pragma unroll
    for (int p = 0; p < 3; ++p) tA[p] = pack8(acc[2 * p], acc[2 * p + 1]);
    const bf16x4 tB = pack4(acc[6]);
    f32x4 sc[5];
#pragma unroll
    for (int nt = 0; nt < 5; ++nt) sc[nt] = (f32x4){0.f, 0.f, 0.f, 0.f};
#pragma unroll
    for (int p = 0; p < 3; ++p)
#pragma unroll
      for (int nt = 0; nt < 5; ++nt) sc[nt] = mfma32(kf[nt][p], tA[p], sc[nt]);
#pragma unroll
    for (int nt = 0; nt < 5; ++nt) sc[nt] = mfma16(kl[nt], tB, sc[nt]);
    ST(c2);
    {
      float* rw = red + (w * 16 + n) * 44;
      *(f32x4*)(rw + 4 * g) = n < 8 ? sc[1] : sc[3];
      *(f32x4*)(rw + 16 + 4 * g) = n < 8 ? sc[2] : sc[4];
      if ((g >> 1) == (n >> 3)) *(f32x4*)(rw + 32 + 4 * (g & 1)) = sc[0];
    }
    __syncthreads();
    ST(c3);
    const bool pre = s2n < 0 || __builtin_amdgcn_readfirstlane((int)(nxc >= 8u)) != 0;
    {
      const int r = 4 * w + g, jj = n < 10 ? n : 9;
      f32x4 sv = *(const f32x4*)(red + r * 44 + 4 * jj);
#pragma unroll
      for (int ww = 1; ww < 4; ++ww) sv += *(const f32x4*)(red + (ww * 16 + r) * 44 + 4 * jj);
      sv *= 0.0625f;
      float mx = fmaxf(fmaxf(sv[0], sv[1]), fmaxf(sv[2], sv[3]));
      mx = fmaxf(mx, ror16<8>(mx)); mx = fmaxf(mx, ror16<4>(mx)); mx = fmaxf(mx, ror16<2>(mx)); mx = fmaxf(mx, ror16<1>(mx));
#pragma unroll
      for (int i = 0; i < 4; ++i) sv[i] = __expf(sv[i] - mx);
      float sum = n < 10 ? (sv[0] + sv[1]) + (sv[2] + sv[3]) : 0.f;
      sum += ror16<8>(sum); sum += ror16<4>(sum); sum += ror16<2>(sum); sum += ror16<1>(sum);
      const float inv = 1.f / sum;
      const unsigned pb = (unsigned)OFF_PB + (tick * NH + h) * 384 * 4;
      const u32x2 pv = {~pk2(sv[0] * inv, sv[1] * inv), ~pk2(sv[2] * inv, sv[3] * inv)};
      if (n < 10) st8(rs, pb + (n < 8 ? r * 64 + n * 8 : 1024 + r * 16 + (n - 8) * 8), pv);
      ST(c4);
      TS((h * 80 + tick) * 4 + 1);
      STS((h * 80 + tick) * 4 + 0, c1 - c0); STS((h * 80 + tick) * 4 + 1, c2 - c1); STS((h * 80 + tick) * 4 + 2, c3 - c2); STS((h * 80 + tick) * 4 + 3, c4 - c3);
    }
    if (tick + 1 < NTICK) dma_xs(ws, lxo, tick + 1, w, 4, lane, !pre);
  }
}

DI void role_V(char* ws, char* smem, int v, const unsigned short* mem0) {
  int tid = threadIdx.x;
  asm volatile("" : "+v"(tid));
  const int lane = tid & 63, w = __builtin_amdgcn_readfirstlane(tid >> 6);
  const int n = lane & 15, g = lane >> 4;
  char* lmem = smem;
  float* red = (float*)(smem + 16 * RS);
  const rsrc_t rs = make_rsrc(ws);
  unsigned* cnt = (unsigned*)(ws + OFF_CNT);
  bf16x8 W[4][14];
#pragma unroll
  for (int hh = 0; hh < 4; ++hh)
#pragma unroll
    for (int ks = 0; ks < 14; ++ks) lda(W[hh][ks], ws + OFF_BF + (((size_t)(4 * w + hh) * 28 + v) * 14 + ks) * 1024 + lane * 16);
  if (tid < 32) {
    const int r = tid >> 1, hf = tid & 1;
    u32x4 m = *(const u32x4*)(mem0 + (size_t)(r & 7) * DM + v * 16 + hf * 8);
    st16(rs, (unsigned)OFF_MEM + (v * 128 + tid * 4) * 4, ~m);
  }
  wait_vm0();
#pragma unroll
  for (int hh = 0; hh < 4; ++hh)
#pragma unroll
    for (int ks = 0; ks < 14; ++ks) asm volatile("" : "+a"(W[hh][ks]));
  u32x4 Wx[2][4], Wn[2][4];
  auto load_wx = [&](int tick, bool wait) {
#pragma unroll
    for (int l = 0; l < 2; ++l) {
      const int sg = l == 0 ? tick : tick - LAG;
      if (sg >= 0 && sg < NSEG) {
        const int task = l * 64 + sg;
        if (wait) wait_cnt(cnt + (C_WX + task) * 32, 16, ws);
        fence_c();
#pragma unroll
        for (int hh = 0; hh < 4; ++hh) Wn[l][hh] = ld16(rs, (unsigned)OFF_WX + ((task * NH + 4 * w + hh) * 28 + v) * 1024 + lane * 16);
      } else {
#pragma unroll
        for (int hh = 0; hh < 4; ++hh) Wn[l][hh] = (u32x4){0u, 0u, 0u, 0u};
      }
    }
  };
  load_wx(0, true);
  const char* mrow = lmem + n * RS + 16 * g;
  for (int tick = 0; tick < NTICK; ++tick) {
    gather_mem(rs, (unsigned)OFF_MEM + tick * MEMW * 4, lmem, tid, ws);
    __syncthreads();
    TS(((16 + v) * 80 + tick) * 4 + 0);
    ST(c0);
#pragma unroll
    for (int l = 0; l < 2; ++l)
#pragma unroll
      for (int hh = 0; hh < 4; ++hh) Wx[l][hh] = Wn[l][hh];
    unsigned nc0 = 16, nc1 = 16;
    {
      const int a0 = tick + 1 < NSEG ? tick + 1 : 0, a1 = (tick + 1 - LAG >= 0 && tick + 1 - LAG < NSEG) ? 64 + tick + 1 - LAG : 0;
      nc0 = ld4(rs, (unsigned)OFF_CNT + (C_WX + a0) * 128); nc1 = ld4(rs, (unsigned)OFF_CNT + (C_WX + a1) * 128);
    }
    SB();
    u32x2 Wm[4];
    {
      bf16x8 mf[14];
#pragma unroll
      for (int ks = 0; ks < 14; ++ks) mf[ks] = *(const bf16x8*)(mrow + 64 * ks);
      SB();
      f32x4 a[4];
#pragma unroll
      for (int hh = 0; hh < 4; ++hh) a[hh] = (f32x4){0.f, 0.f, 0.f, 0.f};
#pragma unroll
      for (int ks = 0; ks < 14; ++ks)
#pragma unroll
        for (int hh = 0; hh < 4; ++hh) a[hh] = mfma32(mf[ks], W[hh][ks], a[hh]);
#pragma unroll
      for (int hh = 0; hh < 4; ++hh) {
        Wm[hh] = __builtin_bit_cast(u32x2, pack4(a[hh]));
        st8(rs, (unsigned)OFF_WM + ((tick * NH + 4 * w + hh) * 28 + v) * 512 + lane * 8, Wm[hh]);
      }
    }
    u32x4 Wl[2][2];
#pragma unroll
    for (int pr = 0; pr < 2; ++pr) {
      const u32x4 q = {Wm[2 * pr][0], Wm[2 * pr][1], Wm[2 * pr + 1][0], Wm[2 * pr + 1][1]}, z4 = {0u, 0u, 0u, 0u};
      Wl[0][pr] = g < 2 ? q : z4; Wl[1][pr] = g < 2 ? z4 : q;
      asm volatile("" : "+v"(Wl[0][pr]), "+v"(Wl[1][pr]));
    }
    ST(c1);
    const bool pre = __builtin_amdgcn_readfirstlane((int)(nc0 >= 16u && nc1 >= 16u)) != 0;
    if (pre && tick + 1 < NTICK) load_wx(tick + 1, false);
    u32x2 pm[4]; u32x4 px[4];
    {
      const unsigned pb = (unsigned)OFF_PB + (tick * NH + 4 * w) * 384 * 4;
      bool all;
      SPIN_DECL;
      do {
        fence_c();
#pragma unroll
        for (int hh = 0; hh < 4; ++hh) { pm[hh] = ld8(rs, pb + hh * 1536 + 1024 + n * 16 + (g & 1) * 8); px[hh] = ld16(rs, pb + hh * 1536 + n * 64 + g * 16); }
        const unsigned m0 = umin(umin(pm[0][0], pm[0][1]), umin(pm[1][0], pm[1][1])), m1 = umin(umin(pm[2][0], pm[2][1]), umin(pm[3][0], pm[3][1]));
        all = umin(umin(m0, m1), umin(umin(mn4(px[0]), mn4(px[1])), umin(mn4(px[2]), mn4(px[3])))) != 0;
        SPIN_CHK(ws, 3);
      } while (!all);
    }
    TS(((16 + v) * 80 + tick) * 4 + 1);
    ST(c2);
    f32x4 o1 = {0.f, 0.f, 0.f, 0.f}, o2 = o1;
#pragma unroll
    for (int pr = 0; pr < 2; ++pr) {
      const bf16x8 pmm = __builtin_bit_cast(bf16x8, not4((u32x4){pm[2 * pr][0], pm[2 * pr][1], pm[2 * pr + 1][0], pm[2 * pr + 1][1]}));
      o1 = mfma32(pmm, __builtin_bit_cast(bf16x8, Wl[0][pr]), o1);
      o2 = mfma32(pmm, __builtin_bit_cast(bf16x8, Wl[1][pr]), o2);
    }
#pragma unroll
    for (int hh = 0; hh < 4; ++hh) {
      const bf16x8 pp = __builtin_bit_cast(bf16x8, not4(px[hh]));
      o1 = mfma32(pp, __builtin_bit_cast(bf16x8, Wx[0][hh]), o1);
      o2 = mfma32(pp, __builtin_bit_cast(bf16x8, Wx[1][hh]), o2);
    }
    const f32x4 o = g < 2 ? o1 : o2;
#pragma unroll
    for (int i = 0; i < 4; ++i) red[(w * 16 + 4 * g + i) * 16 + n] = o[i];
    ST(c3);
    wait_vm0();
    __syncthreads();
    ST(c4);
    if (tid < 32) {
      const int r = tid >> 1, hf = tid & 1;
      f32x4 s0 = {0.f, 0.f, 0.f, 0.f}, s1 = s0;
#pragma unroll
      for (int ww = 0; ww < 4; ++ww) { s0 += *(const f32x4*)(red + (ww * 16 + r) * 16 + hf * 8); s1 += *(const f32x4*)(red + (ww * 16 + r) * 16 + hf * 8 + 4); }
      u32x4 m = {pk2(s0[0], s0[1]), pk2(s0[2], s0[3]), pk2(s1[0], s1[1]), pk2(s1[2], s1[3])};
      if (r >= 8 && tick + 1 <= LAG) m = *(const u32x4*)(mem0 + (size_t)(r & 7) * DM + v * 16 + hf * 8);
      st16(rs, (unsigned)OFF_MEM + ((tick + 1) * MEMW + v * 128 + tid * 4) * 4, ~m);
    } else if (tid == 64) {
      add_cnt(cnt + (C_WM + tick) * 32);
    }
    ST(c5);
    TS(((16 + v) * 80 + tick) * 4 + 2);
    STS(((16 + v) * 80 + tick) * 4 + 0, c1 - c0); STS(((16 + v) * 80 + tick) * 4 + 1, c3 - c2); STS(((16 + v) * 80 + tick) * 4 + 2, c4 - c3); STS(((16 + v) * 80 + tick) * 4 + 3, c5 - c4);
    if (!pre && tick + 1 < NTICK) load_wx(tick + 1, true);
    TS(((16 + v) * 80 + tick) * 4 + 3);
  }
}

DI void load_x(rsrc_t rs, char* lh, unsigned src, int tid) {
  u32x4 v[7];
#pragma unroll
  for (int r = 0; r < 7; ++r) { const int c = tid + 256 * r; v[r] = ld16(rs, src + (c / 56) * DM * 2 + (c % 56) * 16); }
#pragma unroll
  for (int r = 0; r < 7; ++r) { const int c = tid + 256 * r; *(u32x4*)(lh + (c / 56) * RS + (c % 56) * 16) = v[r]; }
}

constexpr int NFA = 50, NFV = 27;
template <bool TR>
DI void gemm_res(const bf16x8 (&Wa)[NFA], const bf16x8 (&Wv)[NFV], const char* lwp, const char* xrow, f32x4 (&acc)[2][7]) {
#pragma unroll
  for (int mt = 0; mt < 2; ++mt)
#pragma unroll
    for (int c = 0; c < 7; ++c) acc[mt][c] = (f32x4){0.f, 0.f, 0.f, 0.f};
  bf16x8 x0[2], x1[2];
  x0[0] = *(const bf16x8*)(xrow); x1[0] = *(const bf16x8*)(xrow + 16 * RS);
  SB();
#pragma unroll
  for (int ks = 0; ks < 14; ++ks) {
    const int cu = ks & 1, nx = cu ^ 1;
    const bf16x8 lf = *(const bf16x8*)(lwp + ks * 1024);
    bf16x8 lg = lf;
    if (ks >= 7) lg = *(const bf16x8*)(lwp + (ks + 7) * 1024);
    if (ks + 1 < 14) { x0[nx] = *(const bf16x8*)(xrow + 64 * (ks + 1)); x1[nx] = *(const bf16x8*)(xrow + 16 * RS + 64 * (ks + 1)); }
    SB();
#pragma unroll
    for (int c = 0; c < 7; ++c) {
      const int f = c * 14 + ks;
      const bf16x8 wf = f < NFA ? Wa[f < NFA ? f : 0] : f < NFA + NFV ? Wv[f < NFA + NFV ? f - NFA : 0] : c == 5 ? lg : lf;
      acc[0][c] = TR ? mfma32(wf, x0[cu], acc[0][c]) : mfma32(x0[cu], wf, acc[0][c]);
      acc[1][c] = TR ? mfma32(wf, x1[cu], acc[1][c]) : mfma32(x1[cu], wf, acc[1][c]);
      if (c == 4) SB();
    }
    SB();
  }
}
DI float wave_sum_dpp(float v) {
  v += ror16<8>(v); v += ror16<4>(v); v += ror16<2>(v); v += ror16<1>(v);
  const int u = __builtin_bit_cast(int, v);
  const float a = __builtin_bit_cast(float, __builtin_amdgcn_readlane(u, 0)), b = __builtin_bit_cast(float, __builtin_amdgcn_readlane(u, 16));
  const float c = __builtin_bit_cast(float, __builtin_amdgcn_readlane(u, 32)), d = __builtin_bit_cast(float, __builtin_amdgcn_readlane(u, 48));
  return (a + b) + (c + d);
}

constexpr int NAW = NTEAM - 3;
DI void role_W(char* ws, char* smem, int j, int h, unsigned short* out) {
  int tid = threadIdx.x;
  asm volatile("" : "+v"(tid));
  const int lane = tid & 63, w = __builtin_amdgcn_readfirstlane(tid >> 6);
  const int n = lane & 15, g = lane >> 4;
  char* lh = smem;
  float* red = (float*)(smem + 48 * RS);
  char* lp = (char*)(red + 4 * 32 * 48);
  char* lw = lp + 32 * 112;
  unsigned* cnt = (unsigned*)(ws + OFF_CNT);
  const rsrc_t rs = make_rsrc(ws);
  for (int i = tid; i < 8 * 57; i += 256) *(u32x4*)(lh + 40 * RS + i * 16) = (u32x4){0u, 0u, 0u, 0u};
  const char* Wf = ws + (j < 3 ? OFF_BF : OFF_AF) + ((size_t)h * 28 + 7 * w) * 14 * 1024 + lane * 16;
  bf16x8 Wa[NFA], Wv[NFV];
#pragma unroll
  for (int ks = 0; ks < 14; ++ks) *(bf16x8*)(lw + (w * 21 + ks) * 1024 + lane * 16) = *(const bf16x8*)(Wf + (6 * 14 + ks) * 1024);
#pragma unroll
  for (int ks = 0; ks < 7; ++ks) *(bf16x8*)(lw + (w * 21 + 14 + ks) * 1024 + lane * 16) = *(const bf16x8*)(Wf + (5 * 14 + 7 + ks) * 1024);
  fence_c();
#pragma unroll
  for (int k = 0; k < NFA; ++k) lda(Wa[k], Wf + k * 1024);
#pragma unroll
  for (int k = 0; k < NFV; ++k) Wv[k] = *(const bf16x8*)(Wf + (NFA + k) * 1024);
  wait_vm0();
#pragma unroll
  for (int k = 0; k < NFA; ++k) asm volatile("" : "+a"(Wa[k]));
  if (j < 3) {
    const int l = j > 0;
    for (int s = l ? j - 1 : 0; s < NSEG; s += l ? 2 : 1) {
      const int task = l * 64 + s;
      asm volatile("" : "+v"(tid));
      const int lane = tid & 63, n = lane & 15, g = lane >> 4;
      const char* lwp = lw + w * 21 * 1024 + lane * 16;
      const char* xrow = lh + n * RS + 16 * g;
      __syncthreads();
      const int tsb = 44 * 80 * 4 + (task * 16 + h) * 8;
      TS(tsb + 0);
      if (l == 1) wait_cnt_wg(cnt + (C_XS2 + s) * 32, 8, ws);
      fence_c();
      load_x(rs, lh, (unsigned)(l == 0 ? OFF_XS1 : OFF_XS2) + s * 32 * DM * 2, tid);
      __syncthreads();
      TS(tsb + 1);
      f32x4 acc[2][7];
      gemm_res<false>(Wa, Wv, lwp, xrow, acc);
      const unsigned wxo = (unsigned)OFF_WX + ((task * NH + h) * 28 + 7 * w) * 1024 + lane * 16;
#pragma unroll
      for (int c = 0; c < 7; ++c) st16(rs, wxo + c * 1024, __builtin_bit_cast(u32x4, pack8(acc[0][c], acc[1][c])));
      wait_vm0();
      __syncthreads();
      if (tid == 0) add_cnt(cnt + (C_WX + task) * 32);
      TS(tsb + 2);
    }
    return;
  }
  int ti = 0;
  for (int key = 0; key < NTICK; ++key) {
    for (int l = 0; l < 2; ++l) {
      const int s = l == 0 ? key : key - LAG;
      if (s < 0 || s >= NSEG) continue;
      const int my = (ti % NAW) == j - 3;
      ++ti;
      if (!my) continue;
      const int task = l * 64 + s;
      asm volatile("" : "+v"(tid));
      const int lane = tid & 63, n = lane & 15, g = lane >> 4;
      const char* lwp = lw + w * 21 * 1024 + lane * 16;
      const char* xrow = lh + n * RS + 16 * g;
      __syncthreads();
      const int tsb = 44 * 80 * 4 + (128 * 16 + task * 16 + h) * 8;
      TS(tsb + 0);
      if (l == 1) wait_cnt_wg(cnt + (C_XS2 + s) * 32, 8, ws);
      fence_c();
      load_x(rs, lh, (unsigned)(l == 0 ? OFF_XS1 : OFF_XS2) + s * 32 * DM * 2, tid);
      __syncthreads();
      TS(tsb + 1);
      bf16x8 tA[2][3]; bf16x4 tB[2];
      {
        f32x4 acc[2][7];
        gemm_res<true>(Wa, Wv, lwp, xrow, acc);
#pragma unroll
        for (int mt = 0; mt < 2; ++mt) {
#pragma unroll
          for (int p = 0; p < 3; ++p) tA[mt][p] = pack8(acc[mt][2 * p], acc[mt][2 * p + 1]);
          tB[mt] = pack4(acc[mt][6]);
        }
      }
      TS(tsb + 2);
      const int tk = l == 0 ? s : s + LAG;
      u32x4 wx[7]; u32x2 wm[7];
      wait_cnt_wg(cnt + (C_WX + task) * 32, 16, ws);
      fence_c();
#pragma unroll
      for (int c = 0; c < 7; ++c) wx[c] = ld16(rs, (unsigned)OFF_WX + ((task * NH + h) * 28 + 7 * w + c) * 1024 + lane * 16);
      wait_cnt_wg(cnt + (C_WM + tk) * 32, NV, ws);
      fence_c();
#pragma unroll
      for (int c = 0; c < 7; ++c) wm[c] = ld8(rs, (unsigned)OFF_WM + ((tk * NH + h) * 28 + 7 * w + c) * 512 + lane * 8);
      {
        const unsigned slot = (unsigned)OFF_MEM + tk * MEMW * 4;
        u32x4 v[2]; unsigned p[2];
#pragma unroll
        for (int r = 0; r < 2; ++r) { int c = tid + 256 * r; if (c >= 448) c = tid; p[r] = slot + ((c >> 4) * 128 + l * 64 + (c & 15) * 4) * 4; }
        bool all;
        SPIN_DECL;
        do {
          fence_c();
#pragma unroll
          for (int r = 0; r < 2; ++r) v[r] = ld16(rs, p[r]);
          all = (zc4(v[0]) | zc4(v[1])) == 0;
          if (!all) __builtin_amdgcn_s_sleep(4);
          SPIN_CHK(ws, 4);
        } while (!all);
#pragma unroll
        for (int r = 0; r < 2; ++r) {
          const int c = tid + 256 * r;
          if (c < 448) { const int vv = c >> 4, row = (c & 15) >> 1, hf = c & 1; *(u32x4*)(lh + (32 + row) * RS + (vv * 16 + hf * 8) * 2) = ~v[r]; }
        }
      }
      __syncthreads();
      TS(tsb + 3);
      {
        f32x4 sacc[2][3];
#pragma unroll
        for (int nt = 0; nt < 3; ++nt) {
          const char* rowp = lh + (16 * nt + n) * RS + 224 * w;
          bf16x8 kf[3];
#pragma unroll
          for (int p = 0; p < 3; ++p) kf[p] = *(const bf16x8*)(rowp + 64 * p + 16 * g);
          const bf16x4 kl = *(const bf16x4*)(rowp + 192 + 8 * g);
#pragma unroll
          for (int mt = 0; mt < 2; ++mt) {
            f32x4 sa = {0.f, 0.f, 0.f, 0.f};
#pragma unroll
            for (int p = 0; p < 3; ++p) sa = mfma32(tA[mt][p], kf[p], sa);
            sacc[mt][nt] = mfma16(tB[mt], kl, sa);
          }
        }
#pragma unroll
        for (int mt = 0; mt < 2; ++mt)
#pragma unroll
          for (int nt = 0; nt < 3; ++nt)
#pragma unroll
            for (int i = 0; i < 4; ++i) red[(w * 32 + 16 * mt + 4 * g + i) * 48 + 16 * nt + n] = sacc[mt][nt][i];
      }
      __syncthreads();
      {
        const int r = tid >> 3, q = tid & 7;
        float sv[5]; float mx = -3.0e38f;
#pragma unroll
        for (int i = 0; i < 5; ++i) {
          const int k = q * 5 + i;
          float a = red[(0 * 32 + r) * 48 + k] + red[(1 * 32 + r) * 48 + k] + red[(2 * 32 + r) * 48 + k] + red[(3 * 32 + r) * 48 + k];
          a *= 0.0625f;
          const bool ok = (k >= 32) || (k <= r);
          sv[i] = ok ? a : -3.0e38f;
          mx = fmaxf(mx, sv[i]);
        }
        mx = fmaxf(mx, __shfl_xor(mx, 1)); mx = fmaxf(mx, __shfl_xor(mx, 2)); mx = fmaxf(mx, __shfl_xor(mx, 4));
        float sum = 0.f;
#pragma unroll
        for (int i = 0; i < 5; ++i) { sv[i] = sv[i] > -1.0e38f ? __expf(sv[i] - mx) : 0.f; sum += sv[i]; }
        sum += __shfl_xor(sum, 1); sum += __shfl_xor(sum, 2); sum += __shfl_xor(sum, 4);
        const float inv = 1.f / sum;
#pragma unroll
        for (int i = 0; i < 5; ++i) { const int k = q * 5 + i; *(unsigned short*)(lp + r * 112 + (k < 32 ? k : k + 8 * l) * 2) = f2bf(sv[i] * inv); }
        if (q == 0) { u32x4 z = {0u, 0u, 0u, 0u}; asm volatile("" : "+v"(z)); *(u32x4*)(lp + r * 112 + 80 - 16 * l) = z; }
      }
      __syncthreads();
      TS(tsb + 4);
      {
        const unsigned part = (unsigned)OFF_PART + (task * NH + h) * (unsigned)SZ_PART1;
#pragma unroll
        for (int mt = 0; mt < 2; ++mt) {
          const char* pr = lp + (16 * mt + n) * 112;
          u32x2 lo = *(const u32x2*)(pr + 8 * g), hi = *(const u32x2*)(pr + 32 + 8 * g);
          const bf16x8 pf = __builtin_bit_cast(bf16x8, (u32x4){lo[0], lo[1], hi[0], hi[1]});
          const bf16x4 pmf = __builtin_bit_cast(bf16x4, *(const u32x2*)(pr + 64 + 8 * g));
#pragma unroll
          for (int c = 0; c < 7; ++c) {
            f32x4 o = {0.f, 0.f, 0.f, 0.f};
            o = mfma32(__builtin_bit_cast(bf16x8, wx[c]), pf, o);
            o = mfma16(__builtin_bit_cast(bf16x4, wm[c]), pmf, o);
            st16(rs, part + (16 * mt + n) * (DM * 4) + (16 * (7 * w + c) + 4 * g) * 4, __builtin_bit_cast(u32x4, o));
          }
        }
      }
      const int row = 4 * (h & 7) + w;
      const bool act = lane < 48;
      const int c0 = 4 * lane, c1 = act ? 256 + 4 * lane : c0;
      const unsigned rbase = (unsigned)((32 * s + row) * DM) * 2;
      u32x2 xr[2], yr[2];
      if (h < 8) {
        const unsigned xo = (unsigned)(l == 0 ? OFF_X1 : OFF_X2) + rbase;
        xr[0] = ld8(rs, xo + c0 * 2); xr[1] = ld8(rs, xo + c1 * 2);
        if (l == 0) { yr[0] = *(const u32x2*)(ws + OFF_Y + rbase + c0 * 2); yr[1] = *(const u32x2*)(ws + OFF_Y + rbase + c1 * 2); }
        else { yr[0] = (u32x2){0u, 0u}; yr[1] = yr[0]; }
      }
      wait_vm0();
      __syncthreads();
      if (tid == 0) add_cnt(cnt + (C_PART + task) * 32);
      TS(tsb + 5);
      if (h < 8) {
        wait_cnt_wg(cnt + (C_PART + task) * 32, 16, ws);
        fence_c();
        TS(tsb + 6);
        f32x4 a[2] = {{0.f, 0.f, 0.f, 0.f}, {0.f, 0.f, 0.f, 0.f}};
        const unsigned p = (unsigned)OFF_PART + task * NH * (unsigned)SZ_PART1 + row * (DM * 4);
#pragma unroll
        for (int hb = 0; hb < 2; ++hb) {
          u32x4 v[8][2];
#pragma unroll
          for (int hh = 0; hh < 8; ++hh) { v[hh][0] = ld16(rs, p + (8 * hb + hh) * (unsigned)SZ_PART1 + c0 * 4); v[hh][1] = ld16(rs, p + (8 * hb + hh) * (unsigned)SZ_PART1 + c1 * 4); }
#pragma unroll
          for (int hh = 0; hh < 8; ++hh) { a[0] += __builtin_bit_cast(f32x4, v[hh][0]); a[1] += __builtin_bit_cast(f32x4, v[hh][1]); }
        }
        float xv[2][4];
#pragma unroll
        for (int q = 0; q < 2; ++q)
#pragma unroll
          for (int i = 0; i < 4; ++i) {
            const unsigned u = xr[q][i >> 1];
            xv[q][i] = rbf(bf2f((unsigned short)((i & 1) ? u >> 16 : u & 0xffff)) + rbf(a[q][i]));
          }
        if (l == 1) {
          *(u32x2*)((char*)out + rbase + c0 * 2) = (u32x2){pk2(xv[0][0], xv[0][1]), pk2(xv[0][2], xv[0][3])};
          if (act) *(u32x2*)((char*)out + rbase + c1 * 2) = (u32x2){pk2(xv[1][0], xv[1][1]), pk2(xv[1][2], xv[1][3])};
        } else {
#pragma unroll
          for (int pass = 0; pass < 2; ++pass) {
            const float s0 = (xv[0][0] + xv[0][1]) + (xv[0][2] + xv[0][3]), s1 = (xv[1][0] + xv[1][1]) + (xv[1][2] + xv[1][3]);
            const float mean = wave_sum_dpp(act ? s0 + s1 : s0) * (1.f / DM);
            float q0 = 0.f, q1 = 0.f;
#pragma unroll
            for (int i = 0; i < 4; ++i) { const float d0 = xv[0][i] - mean, d1 = xv[1][i] - mean; q0 += d0 * d0; q1 += d1 * d1; }
            const float var = wave_sum_dpp(act ? q0 + q1 : q0) * (1.f / DM);
            const float mb = rbf(mean), den = rbf(sqrtf(rbf(rbf(var) + 1e-5f)));
            float ln[2][4];
#pragma unroll
            for (int q = 0; q < 2; ++q)
#pragma unroll
              for (int i = 0; i < 4; ++i) {
                ln[q][i] = rbf(rbf(xv[q][i] - mb) / den);
                if (pass == 0) {
                  const unsigned u = yr[q][i >> 1];
                  xv[q][i] = rbf(xv[q][i] + rbf(ln[q][i] * bf2f((unsigned short)((i & 1) ? u >> 16 : u & 0xffff))));
                }
              }
            const unsigned dst = (unsigned)(pass == 0 ? OFF_X2 : OFF_XS2) + rbase;
            if (pass == 0) {
              st8(rs, dst + c0 * 2, (u32x2){pk2(xv[0][0], xv[0][1]), pk2(xv[0][2], xv[0][3])});
              if (act) st8(rs, dst + c1 * 2, (u32x2){pk2(xv[1][0], xv[1][1]), pk2(xv[1][2], xv[1][3])});
            } else {
              st8(rs, dst + c0 * 2, (u32x2){pk2(ln[0][0], ln[0][1]), pk2(ln[0][2], ln[0][3])});
              if (act) st8(rs, dst + c1 * 2, (u32x2){pk2(ln[1][0], ln[1][1]), pk2(ln[1][2], ln[1][3])});
            }
          }
          wait_vm0();
          __syncthreads();
          if (tid == 0) add_cnt(cnt + (C_XS2 + s) * 32);
        }
        TS(tsb + 7);
      }
    }
  }
}

__global__ void __launch_bounds__(256, 1) k_main(char* __restrict__ ws, const unsigned short* __restrict__ mem0, unsigned short* __restrict__ out) {
  extern __shared__ __attribute__((aligned(16))) char smem[];
  const int b = blockIdx.x;
  if (b < 16) role_S(ws, smem, b);
  else if (b < 16 + NV) role_V(ws, smem, b - 16, mem0);
  else { const int k = b - 16 - NV; role_W(ws, smem, k >> 4, k & 15, out); }
}

extern "C" void kernel_launch(void* const* d_in, const int* in_sizes, int n_in, void* d_out, int out_size, void* d_ws,
                              size_t ws_size, hipStream_t stream) {
  const unsigned short* A_q = (const unsigned short*)d_in[0];
  const unsigned short* A_k = (const unsigned short*)d_in[1];
  const unsigned short* A_v = (const unsigned short*)d_in[2];
  const unsigned short* A_shift = (const unsigned short*)d_in[3];
  const unsigned short* A_ssmk = (const unsigned short*)d_in[4];
  const unsigned short* A_D = (const unsigned short*)d_in[5];
  const unsigned short* B_mem0 = (const unsigned short*)d_in[7];
  const unsigned short* B_Wq = (const unsigned short*)d_in[8];
  const unsigned short* B_Wk = (const unsigned short*)d_in[9];
  const unsigned short* B_Wv = (const unsigned short*)d_in[10];
  const unsigned short* B_Wo = (const unsigned short*)d_in[11];
  char* ws = (char*)d_ws;
  constexpr int LDS_PRE = (2 * 8 * HC_STRIDE + 2 * KV_CH) * 2 + (2048 + 8) * 4 + 2 * 2128 * 2;
  constexpr int LDS_S = 80 * RS + 4 * 14 * 1024 + 4 * 5 * 16 * 16 * 4;
  constexpr int LDS_V = 144 * RS + 4 * 16 * 16 * 4;
  constexpr int LDS_W = 48 * RS + 4 * 32 * 48 * 4 + 32 * 112 + 84 * 1024;
  constexpr int LDS_MAIN = LDS_S > LDS_W ? LDS_S : LDS_W;
  hipFuncSetAttribute((const void*)k_pre, hipFuncAttributeMaxDynamicSharedMemorySize, LDS_PRE);
  hipFuncSetAttribute((const void*)k_main, hipFuncAttributeMaxDynamicSharedMemorySize, LDS_MAIN);
  k_pre<<<224, 256, LDS_PRE, stream>>>(A_k, A_v, A_shift, A_ssmk, A_D, ws);
  k_ln<<<SEQ / 4 + 224, 256, 0, stream>>>(A_q, B_Wq, B_Wk, B_Wv, B_Wo, ws);
  k_main<<<16 + NV + 16 * NTEAM, 256, LDS_MAIN, stream>>>(ws, B_mem0, (unsigned short*)d_out);
}
```

```cpp
#include <hip/hip_runtime.h>
#include <hip/hip_bf16.h>
#include <stdint.h>

typedef __attribute__((ext_vector_type(8))) short bf16x8;
typedef __attribute__((ext_vector_type(4))) short bf16x4;
typedef __attribute__((ext_vector_type(4))) float f32x4;
typedef __attribute__((ext_vector_type(16))) float f32x16;
typedef __attribute__((ext_vector_type(4))) unsigned u32x4;
typedef __attribute__((ext_vector_type(2))) unsigned u32x2;
typedef __attribute__((ext_vector_type(2))) float f32x2;
typedef __attribute__((ext_vector_type(2))) __bf16 bf2;
#define DI __device__ __forceinline__
#define GAS __attribute__((address_space(1)))

constexpr int DM = 448, SEQ = 2048, NH = 16, NSEG = 64;
#define WSLP 8
#define PSLP 24
#define MSLP0 0
#define MSLP 16
#define CX 4
#define SMW 0
#define NCW 2
#define NWX1 1
constexpr int NTEAM = NCW + (256 - 32 * CX) / 16;
constexpr int NV = 28;
constexpr int NSV = 2 * (NH + NV);
static_assert(NSV + 16 * NCW <= 32 * CX, "placement");
constexpr int NWXT = 1 + NWX1;
constexpr int NAW = NTEAM - NWXT;
#define NAW0V ((NAW + 1) / 2)
constexpr int NAW0 = NAW0V, NAW1 = NAW - NAW0;
constexpr int RS = 912;
constexpr int MEMB = NV * 256;
constexpr int PBB = 640;

constexpr size_t SZ_F = (size_t)NH * 28 * 14 * 1024;
constexpr size_t SZ_X = (size_t)SEQ * DM * 2;
constexpr size_t OFF_AF = 0;
constexpr size_t OFF_BF = OFF_AF + SZ_F;
constexpr size_t OFF_Y = OFF_BF + SZ_F;
constexpr size_t OFF_X1 = OFF_Y + SZ_X;
constexpr size_t OFF_XS1 = OFF_X1 + SZ_X;
constexpr size_t OFF_X2 = OFF_XS1 + SZ_X;
constexpr size_t OFF_XS2 = OFF_X2 + SZ_X;
constexpr size_t OFF_Z = OFF_XS2 + SZ_X;
constexpr size_t OFF_MEM = OFF_Z;
constexpr size_t SZ_MEM = (size_t)128 * MEMB;
constexpr size_t OFF_PB = OFF_MEM + SZ_MEM;
constexpr size_t SZ_PB = (size_t)128 * NH * PBB;
constexpr size_t OFF_WM = OFF_PB + SZ_PB;
constexpr int SZ_WM1 = NH * 28 * 256;
constexpr size_t OFF_CNT = OFF_WM + (size_t)128 * SZ_WM1;
constexpr int C_PART = 0, C_XS2 = 128, C_WX = 192, C_WM = 320, C_ABORT = 448, C_XS1 = 450, C_LNALL = 514;
constexpr size_t SZ_CNT = (size_t)516 * 128;
constexpr size_t OFF_ZEND = OFF_CNT + SZ_CNT;
constexpr size_t SZ_PART1 = (size_t)28 * 2 * 64 * 16;
constexpr size_t OFF_PART = (OFF_ZEND + 4095) / 4096 * 4096;
constexpr size_t OFF_WX = OFF_PART + (size_t)128 * NH * SZ_PART1;
constexpr size_t OFF_END = OFF_WX + (size_t)128 * NH * 28 * 1024;
static_assert(OFF_END <= ((size_t)240 << 20), "workspace");
static_assert(OFF_Z % 16 == 0 && (OFF_ZEND - OFF_Z) % 16 == 0, "zero region");

DI float bf2f(unsigned short u) { return __uint_as_float((unsigned)u << 16); }
DI unsigned short f2bf(float f) { __bf16 b = (__bf16)f; return __builtin_bit_cast(unsigned short, b); }
DI float rbf(float f) { return bf2f(f2bf(f)); }
DI unsigned pk2(float a, float b) { bf2 v = {(__bf16)a, (__bf16)b}; return __builtin_bit_cast(unsigned, v); }
DI f32x4 mfma32(bf16x8 a, bf16x8 b, f32x4 c) { return __builtin_amdgcn_mfma_f32_16x16x32_bf16(a, b, c, 0, 0, 0); }
DI f32x4 mfma16(bf16x4 a, bf16x4 b, f32x4 c) {
  const bf16x8 a8 = {a[0], a[1], a[2], a[3], 0, 0, 0, 0}, b8 = {b[0], b[1], b[2], b[3], 0, 0, 0, 0};
  return __builtin_amdgcn_mfma_f32_16x16x32_bf16(a8, b8, c, 0, 0, 0);
}
typedef __attribute__((ext_vector_type(16))) short bf16x16;
typedef __attribute__((ext_vector_type(8))) __bf16 hb8;
typedef __attribute__((ext_vector_type(16))) __bf16 hb16;
DI f32x4 smfma(bf16x8 a, bf16x16 b, f32x4 c, int idx) {
  return __builtin_amdgcn_smfmac_f32_16x16x64_bf16(__builtin_bit_cast(hb8, a), __builtin_bit_cast(hb16, b), c, idx, 0, 0);
}
DI bf16x16 cat16(bf16x8 a, bf16x8 b) { return __builtin_shufflevector(a, b, 0, 1, 2, 3, 4, 5, 6, 7, 8, 9, 10, 11, 12, 13, 14, 15); }
DI bf16x4 ld_tr(const char* p) { return __builtin_amdgcn_ds_read_tr16_b64_v4i16((__attribute__((address_space(3))) bf16x4*)(const __attribute__((address_space(3))) char*)p); }
DI bf16x4 pack4(f32x4 v) { u32x2 p = {pk2(v[0], v[1]), pk2(v[2], v[3])}; return __builtin_bit_cast(bf16x4, p); }
DI bf16x8 pack8(f32x4 a, f32x4 b) { u32x4 p = {pk2(a[0], a[1]), pk2(a[2], a[3]), pk2(b[0], b[1]), pk2(b[2], b[3])}; return __builtin_bit_cast(bf16x8, p); }

typedef __amdgpu_buffer_rsrc_t rsrc_t;
constexpr int AUX_SC1 = 16;
DI rsrc_t make_rsrc(const void* p) { return __builtin_amdgcn_make_buffer_rsrc((void*)p, (short)0, (int)0x10000000, 0x00020000); }
DI u32x4 ld16(rsrc_t r, unsigned off) { return __builtin_amdgcn_raw_buffer_load_b128(r, off, 0, AUX_SC1); }
DI u32x2 ld8(rsrc_t r, unsigned off) { return __builtin_amdgcn_raw_buffer_load_b64(r, off, 0, AUX_SC1); }
DI unsigned ld4(rsrc_t r, unsigned off) { return __builtin_amdgcn_raw_buffer_load_b32(r, off, 0, AUX_SC1); }
DI void st16(rsrc_t r, unsigned off, u32x4 v) { __builtin_amdgcn_raw_buffer_store_b128(v, r, off, 0, AUX_SC1); }
DI void st8(rsrc_t r, unsigned off, u32x2 v) { __builtin_amdgcn_raw_buffer_store_b64(v, r, off, 0, AUX_SC1); }
DI void st4(rsrc_t r, unsigned off, unsigned v) { __builtin_amdgcn_raw_buffer_store_b32(v, r, off, 0, AUX_SC1); }
DI void SB() { __builtin_amdgcn_sched_barrier(0); }
DI void dma16(const void* gp, unsigned lds_off) {
  asm volatile("s_mov_b32 m0, %1\n s_nop 0\n global_load_lds_dwordx4 %0, off sc1" ::"v"(gp), "s"(lds_off) : "memory");
}
DI unsigned lds_off(const void* p) { return (unsigned)(size_t)(const __attribute__((address_space(3))) void*)p; }
DI void lda(bf16x8& d, const void* p) { asm volatile("global_load_dwordx4 %0, %1, off" : "=a"(d) : "v"(p)); }
DI void ldw7(bf16x8 (&d)[7], unsigned v0, unsigned v1, const char* base) {
  asm volatile(
      "s_nop 4\n"
      "global_load_dwordx4 %0, %7, %9\n"
      "global_load_dwordx4 %1, %7, %9 offset:1024\n"
      "global_load_dwordx4 %2, %7, %9 offset:2048\n"
      "global_load_dwordx4 %3, %7, %9 offset:3072\n"
      "global_load_dwordx4 %4, %8, %9\n"
      "global_load_dwordx4 %5, %8, %9 offset:1024\n"
      "global_load_dwordx4 %6, %8, %9 offset:2048\n"
      : "=&v"(d[0]), "=&v"(d[1]), "=&v"(d[2]), "=&v"(d[3]), "=&v"(d[4]), "=&v"(d[5]), "=&v"(d[6])
      : "v"(v0), "v"(v1), "s"(base));
}
template <int N> DI void waitw7(bf16x8 (&d)[7]) {
  asm volatile("s_waitcnt vmcnt(%7)" : "+v"(d[0]), "+v"(d[1]), "+v"(d[2]), "+v"(d[3]), "+v"(d[4]), "+v"(d[5]), "+v"(d[6]) : "n"(N));
}
template <int N> DI float ror16(float v) { return __builtin_bit_cast(float, __builtin_amdgcn_update_dpp(0, __builtin_bit_cast(int, v), 0x120 + N, 0xf, 0xf, false)); }
DI void fence_c() { asm volatile("" ::: "memory"); }
DI void wait_vm0() { asm volatile("s_waitcnt vmcnt(0)" ::: "memory"); }
DI unsigned zc4(u32x4 v) { return (unsigned)(v[0] == 0u) | (unsigned)(v[1] == 0u) | (unsigned)(v[2] == 0u) | (unsigned)(v[3] == 0u); }
DI unsigned zc2(u32x2 v) { return (unsigned)(v[0] == 0u) | (unsigned)(v[1] == 0u); }
DI unsigned umin(unsigned a, unsigned b) { return a < b ? a : b; }
DI unsigned mn4(u32x4 v) { return umin(umin(v[0], v[1]), umin(v[2], v[3])); }
DI unsigned nz(unsigned u) { return u ? u : 0x8000u; }
DI u32x2 nz2(u32x2 v) { return (u32x2){nz(v[0]), nz(v[1])}; }
template <int N> DI float max_ror16(float v) {
  float r;
  asm("s_nop 1\n v_max_f32_dpp %0, %1, %1 row_ror:%2 row_mask:0xf bank_mask:0xf" : "=v"(r) : "v"(v), "n"(N));
  return r;
}
DI unsigned ld_cnt(const unsigned* p) { return __hip_atomic_load((const GAS unsigned*)p, __ATOMIC_RELAXED, __HIP_MEMORY_SCOPE_AGENT); }
DI void add_cnt(unsigned* p) { __hip_atomic_fetch_add((GAS unsigned*)p, 1u, __ATOMIC_RELAXED, __HIP_MEMORY_SCOPE_AGENT); }
#define SPIN_DECL
#define SPIN_CHK(ws, code)
#define TS(slot)
#define TSW(slot)
#define ST(var)
#define PTS(slot)
#define FTS(slot)
#define STS(slot, val)
#define TL(slot)
#define TLV(slot, v)
#define LT(var)
#define LW(slot, var)
#define LDUMP(base)
#define WXSL 8
#define OLDP
#define PFBAR
#define MDLY 130
#define PDLY 135
#define VMD 35
DI unsigned rtime() { return (unsigned)__builtin_amdgcn_s_memrealtime(); }
DI void delay_until(unsigned tref, unsigned d) { while (rtime() - tref < d) __builtin_amdgcn_s_sleep(1); }
DI void wait_cnt(const unsigned* p, unsigned target, const char* ws) {
  SPIN_DECL;
  while (ld_cnt(p) < target) { __builtin_amdgcn_s_sleep(8); SPIN_CHK((char*)ws, 1); }
}
#define WSL 24
template <int SLP = WSL>
DI void wait_cnt_wg(const unsigned* p, unsigned target, const char* ws) {
  if (threadIdx.x < 64) {
    SPIN_DECL;
    while (ld_cnt(p) < target) { __builtin_amdgcn_s_sleep(SLP); SPIN_CHK((char*)ws, 1); }
  }
  __syncthreads();
}

DI float wave_sum(float v) {
#pragma unroll
  for (int o = 32; o > 0; o >>= 1) v += __shfl_xor(v, o);
  return v;
}

DI void bar4(unsigned* c, unsigned target, int lane) {
  if (lane == 0) __hip_atomic_fetch_add(c, 1u, __ATOMIC_RELEASE, __HIP_MEMORY_SCOPE_WORKGROUP);
  while (__hip_atomic_load(c, __ATOMIC_ACQUIRE, __HIP_MEMORY_SCOPE_WORKGROUP) < target) __builtin_amdgcn_s_sleep(1);
}
#define FD 4
#define ZST 0
#define CLD 0
template <int mat>
DI void fold_unit(int u4, int kq, int lane, const unsigned short* __restrict__ Wq, const unsigned short* __restrict__ Wk,
                  const unsigned short* __restrict__ Wv, const unsigned short* __restrict__ Wo, char* __restrict__ ws, char* lb, unsigned* bc) {
  const int rem = u4 % 112, h = rem / 7, cg = rem % 7;
  const int n = lane & 15, g = lane >> 4;
  const unsigned short* Wa = mat == 0 ? Wq : Wv;
  const unsigned short* pa = Wa + ((size_t)(16 * 7 * kq + n) * NH + h) * 256 + 8 * g;
  bf16x8 af[FD][8];
  if (mat == 0) {
    const int ct = 4 * cg + kq;
    const int wq = ct / 7, j = ct % 7;
    const int cc = j < 6 ? 112 * wq + 32 * (j >> 1) + 8 * (n >> 2) + 4 * (j & 1) + (n & 3) : 112 * wq + 96 + n;
    const unsigned short* p = Wk + ((size_t)cc * NH + h) * 256 + 8 * g;
    bf16x8 b[8];
#pragma unroll
    for (int es = 0; es < 8; ++es) b[es] = *(const bf16x8*)(p + 32 * es);
    SB();
#pragma unroll
    for (int k7 = 0; k7 < FD; ++k7)
#pragma unroll
      for (int es = 0; es < 8; ++es) af[k7][es] = *(const bf16x8*)(pa + (size_t)k7 * 16 * NH * 256 + 32 * es);
    SB();
#pragma unroll
    for (int es = 0; es < 8; ++es) *(bf16x8*)(lb + (kq * 8 + es) * 1024 + lane * 16) = b[es];
  } else {
    const char* pb = (const char*)(Wo + ((size_t)h * 256 + 4) * DM + 64 * cg);
    const unsigned lo = (unsigned)(8 * g * DM + 4 * n) * 2;
    u32x2 r[2][8];
#pragma unroll
    for (int e2 = 0; e2 < 2; ++e2)
#pragma unroll
      for (int i = 0; i < 8; ++i) r[e2][i] = *(const u32x2*)(pb + (2 * kq + e2) * (32 * DM * 2) + (size_t)lo + (i - 4) * (DM * 2));
    SB();
#pragma unroll
    for (int k7 = 0; k7 < FD; ++k7)
#pragma unroll
      for (int es = 0; es < 8; ++es) af[k7][es] = *(const bf16x8*)(pa + (size_t)k7 * 16 * NH * 256 + 32 * es);
    SB();
#pragma unroll
    for (int e2 = 0; e2 < 2; ++e2)
#pragma unroll
      for (int j = 0; j < 4; ++j) {
        u32x4 f;
#pragma unroll
        for (int d = 0; d < 4; ++d) f[d] = __builtin_amdgcn_perm(r[e2][2 * d + 1][j >> 1], r[e2][2 * d][j >> 1], (j & 1) ? 0x07060302u : 0x05040100u);
        *(u32x4*)(lb + (j * 8 + 2 * kq + e2) * 1024 + lane * 16) = f;
      }
  }
  FTS(1);
  bar4(bc, 4, lane);
  FTS(2);
  unsigned short* dst = (unsigned short*)(ws + (mat == 0 ? OFF_AF : OFF_BF)) + ((size_t)(h * 28 + 4 * cg) * 14) * 512;
  auto put = [&](int k7, int c4, f32x4 acc) {
    const int kt = 7 * kq + k7;
    const int ks = kt >> 1, gp = 2 * (kt & 1) + (g >> 1), hf = g & 1;
    const int oc = mat == 0 ? c4 : n >> 2, on = mat == 0 ? n : 4 * (n & 3) + c4;
    *(bf16x4*)(dst + (size_t)(oc * 14 + ks) * 512 + (gp * 16 + on) * 8 + hf * 4) = pack4(acc);
  };
  const char* lbl = lb + lane * 16;
#pragma unroll
  for (int ps = 0; ps < 2; ++ps) {
    const int nt = ps ? 3 : 4;
    f32x4 acc[4][4];
#pragma unroll
    for (int t = 0; t < 4; ++t)
#pragma unroll
      for (int c4 = 0; c4 < 4; ++c4) acc[t][c4] = (f32x4){0.f, 0.f, 0.f, 0.f};
    bf16x8 bb[2][4];
#pragma unroll
    for (int c4 = 0; c4 < 4; ++c4) bb[0][c4] = *(const bf16x8*)(lbl + c4 * 8192);
#pragma unroll
    for (int es = 0; es < 8; ++es) {
      if (es + 1 < 8) {
#pragma unroll
        for (int c4 = 0; c4 < 4; ++c4) bb[(es + 1) & 1][c4] = *(const bf16x8*)(lbl + (c4 * 8 + es + 1) * 1024);
      }
      SB();
#pragma unroll
      for (int c4 = 0; c4 < 4; ++c4)
#pragma unroll
        for (int t = 0; t < nt; ++t) acc[t][c4] = mfma32(af[t][es], bb[es & 1][c4], acc[t][c4]);
      SB();
      if (ps == 0) {
#pragma unroll
        for (int t = 0; t < 3; ++t) af[t][es] = *(const bf16x8*)(pa + (size_t)(t + 4) * 16 * NH * 256 + 32 * es);
        SB();
      }
    }
    if (ps == 0) FTS(3);
#pragma unroll
    for (int t = 0; t < nt; ++t)
#pragma unroll
      for (int c4 = 0; c4 < 4; ++c4) put(4 * ps + t, c4, acc[t][c4]);
    if (ps == 0) FTS(4);
    if (ps == 1) FTS(5);
  }
}

constexpr int HC_STRIDE = 2192;
constexpr int KV_CH = 63 * 72;
constexpr int LDS_CONV = (2 * 8 * HC_STRIDE + 2 * KV_CH) * 2 + 2 * 2128 * 2 + 16 * 1024;
constexpr int LDS_PRE = LDS_CONV + 32 * 1024 + 16;
__global__ void __launch_bounds__(512) k_pre(const unsigned short* __restrict__ gk, const unsigned short* __restrict__ gv,
                                             const unsigned short* __restrict__ gshift,
                                             const unsigned short* __restrict__ gssmk, const unsigned short* __restrict__ gD,
                                             const unsigned short* __restrict__ Wq,
                                             const unsigned short* __restrict__ Wk, const unsigned short* __restrict__ Wv,
                                             const unsigned short* __restrict__ Wo, char* __restrict__ ws) {
  extern __shared__ __attribute__((aligned(16))) char smem[];
  const int tid = threadIdx.x, lane = tid & 63, w = __builtin_amdgcn_readfirstlane(tid >> 6);
  unsigned* bc = (unsigned*)(smem + LDS_PRE - 16);
  if (tid == 0) { bc[0] = 0; bc[1] = 0; }
  TL(4096 + blockIdx.x);
  __syncthreads();
  if (w >= 4) {
    const int fu = ((blockIdx.x & 7) * 4 + (blockIdx.x >> 3) / 7) * 7 + (blockIdx.x >> 3) % 7;
    if (fu < 112) fold_unit<0>(fu, w - 4, lane, Wq, Wk, Wv, Wo, ws, smem + LDS_CONV, bc + 1);
    else fold_unit<1>(fu, w - 4, lane, Wq, Wk, Wv, Wo, ws, smem + LDS_CONV, bc + 1);
    return;
  }
  {
    const int d0 = ((blockIdx.x & 7) * 28 + (blockIdx.x >> 3)) * 2;
    unsigned short* hc = (unsigned short*)smem;
    unsigned short* kvz = hc + 2 * 8 * HC_STRIDE;
    unsigned short* hr = kvz + 2 * KV_CH;
    float* ex = (float*)(hr + 2 * 2128);
    PTS(0);
    {
      const int ch = tid >> 7, i0 = (tid & 127) * 16;
      const u32x4 a = *(const u32x4*)(gssmk + (size_t)(d0 + ch) * SEQ + i0), b = *(const u32x4*)(gssmk + (size_t)(d0 + ch) * SEQ + i0 + 8);
      const int t0 = tid * 8;
      unsigned kr[11], vr[8];
#pragma unroll
      for (int i = 0; i < 11; ++i) { const int t = t0 - 3 + i; kr[i] = *(const unsigned*)(gk + (size_t)(t < 0 ? 0 : t) * DM + d0); }
#pragma unroll
      for (int i = 0; i < 8; ++i) vr[i] = *(const unsigned*)(gv + (size_t)(t0 + i) * DM + d0);
      const u32x4 sr = *(const u32x4*)(gshift + 4 * d0);
      fence_c();
      for (size_t i = (size_t)blockIdx.x * 256 + tid; i < (OFF_WM - OFF_Z) / 16; i += 224 * 256) *(u32x4*)(ws + OFF_Z + i * 16) = (u32x4){0u, 0u, 0u, 0u};
      for (size_t i = (size_t)blockIdx.x * 256 + tid; i < (OFF_ZEND - OFF_CNT) / 16; i += 224 * 256) *(u32x4*)(ws + OFF_CNT + i * 16) = (u32x4){0u, 0u, 0u, 0u};
      for (int i = blockIdx.x * 256 + tid; i < 2 * SZ_WM1 / 16; i += 224 * 256)
        *(u32x4*)(ws + OFF_WM + (size_t)(i / (SZ_WM1 / 16)) * NSEG * SZ_WM1 + (i % (SZ_WM1 / 16)) * 16) = (u32x4){0u, 0u, 0u, 0u};
      fence_c();
      for (int i = tid; i < 2 * 279; i += 256) *(u32x4*)(kvz + (i / 279) * KV_CH + (i % 279) * 8) = (u32x4){0u, 0u, 0u, 0u};
      if (tid == 0) { kr[0] = 0u; kr[1] = 0u; kr[2] = 0u; }
#pragma unroll
      for (int c2 = 0; c2 < 2; ++c2) {
        auto el = [&](unsigned u) { return bf2f((unsigned short)(c2 ? u >> 16 : u & 0xffff)); };
        float o[8];
#pragma unroll
        for (int r = 0; r < 8; ++r) {
          float acc = 0.f;
#pragma unroll
          for (int j = 0; j < 4; ++j) {
            const unsigned su = sr[2 * c2 + ((3 - j) >> 1)];
            acc = rbf(acc + rbf(el(kr[r + j]) * bf2f((unsigned short)(((3 - j) & 1) ? su >> 16 : su & 0xffff))));
          }
          o[r] = acc * el(vr[r]);
        }
        *(u32x4*)(kvz + c2 * KV_CH + (31 + (t0 >> 6)) * 72 + (t0 & 63)) = (u32x4){pk2(o[0], o[1]), pk2(o[2], o[3]), pk2(o[4], o[5]), pk2(o[6], o[7])};
      }
      auto sw = [](unsigned x) { return (x >> 16) | (x << 16); };
      *(u32x4*)(hr + ch * 2128 + 2040 - i0) = (u32x4){sw(a[3]), sw(a[2]), sw(a[1]), sw(a[0])};
      *(u32x4*)(hr + ch * 2128 + 2032 - i0) = (u32x4){sw(b[3]), sw(b[2]), sw(b[1]), sw(b[0])};
      if (tid < 20) *(u32x4*)(hr + (tid / 10) * 2128 + 2048 + (tid % 10) * 8) = (u32x4){0u, 0u, 0u, 0u};
    }
    bar4(bc, 4, lane);
    TL(4864 + blockIdx.x);
    PTS(1);
    for (int it = tid; it < 2 * 264; it += 256) {
      const int ch = it / 264, u = (it % 264) * 8;
      const u32x4 a = *(const u32x4*)(hr + ch * 2128 + u), b = *(const u32x4*)(hr + ch * 2128 + u + 8);
      const unsigned d[8] = {a[0], a[1], a[2], a[3], b[0], b[1], b[2], b[3]};
#pragma unroll
      for (int q = 0; q < 8; ++q) {
        u32x4 o;
#pragma unroll
        for (int k = 0; k < 4; ++k) o[k] = (q & 1) ? __builtin_amdgcn_alignbyte(d[(q >> 1) + k + 1], d[(q >> 1) + k], 2) : d[(q >> 1) + k];
        *(u32x4*)(hc + (ch * 8 + q) * HC_STRIDE + u) = o;
      }
    }
    bar4(bc, 8, lane);
    PTS(3);
    {
      const int ch = w >> 1, hv = w & 1;
      const int n = lane & 31, hf = lane >> 5;
      f32x16 acc[2];
#pragma unroll
      for (int i = 0; i < 16; ++i) { acc[0][i] = 0.f; acc[1][i] = 0.f; }
      const int q = (2047 - n) & 7;
      const unsigned short* hcc = hc + (ch * 8 + q) * HC_STRIDE + (2047 - n - q) + 8 * hf - 1024 * hv;
      const unsigned short* kz = kvz + ch * KV_CH + 31 * 72;
      const unsigned short* kzz = kz + 72 * (n - 16 * hv) + 8 * hf;
      bf16x8 A[2][6], B[2][4];
#pragma unroll
      for (int e = 0; e < 6; ++e) A[0][e] = *(const bf16x8*)(hcc + 16 * (e - 2));
#pragma unroll
      for (int ks = 0; ks < 4; ++ks) B[0][ks] = *(const bf16x8*)(kzz + 16 * ks);
#pragma unroll 2
      for (int cc = 0; cc < 16; ++cc) {
        const int cu = cc & 1, nx = cu ^ 1;
        if (cc + 1 < 16) {
#pragma unroll
          for (int e = 0; e < 4; ++e) A[nx][e] = *(const bf16x8*)(hcc + 16 * (e - 2) - 64 * (cc + 1));
#pragma unroll
          for (int ks = 0; ks < 4; ++ks) B[nx][ks] = *(const bf16x8*)(kzz + 16 * ks - 72 * (cc + 1));
        }
        A[nx][4] = A[cu][0]; A[nx][5] = A[cu][1];
        SB();
#pragma unroll
        for (int ks = 0; ks < 4; ++ks) {
          acc[0] = __builtin_amdgcn_mfma_f32_32x32x16_bf16(A[cu][ks + 2], B[cu][ks], acc[0], 0, 0, 0);
          acc[1] = __builtin_amdgcn_mfma_f32_32x32x16_bf16(A[cu][ks], B[cu][ks], acc[1], 0, 0, 0);
        }
        SB();
      }
      PTS(4);
      TL(5120 + blockIdx.x);
      {
        const f32x16 o = hv ? acc[0] : acc[1];
#pragma unroll
        for (int i = 0; i < 4; ++i) *(f32x4*)(ex + ((w * 4 + i) * 64 + lane) * 4) = (f32x4){o[4 * i], o[4 * i + 1], o[4 * i + 2], o[4 * i + 3]};
      }
      bar4(bc, 12, lane);
      f32x16 fin = hv ? acc[1] : acc[0];
#pragma unroll
      for (int i = 0; i < 4; ++i) {
        const f32x4 p = *(const f32x4*)(ex + (((w ^ 1) * 4 + i) * 64 + lane) * 4);
#pragma unroll
        for (int k = 0; k < 4; ++k) fin[4 * i + k] += p[k];
      }
      const float Dv = bf2f(gD[d0 + ch]);
      unsigned short* yb = hr;
      u32x2 kq4[4];
#pragma unroll
      for (int i = 0; i < 4; ++i) kq4[i] = *(const u32x2*)(kz + n * 72 + 32 * hv + 8 * i + 4 * hf);
#pragma unroll
      for (int r = 0; r < 16; ++r) {
        const int row = (r & 3) + 8 * (r >> 2) + 4 * hf;
        const int t = 64 * n + 32 * hv + row;
        const unsigned u = kq4[r >> 2][(r >> 1) & 1];
        const float kvv = bf2f((unsigned short)((r & 1) ? u >> 16 : u & 0xffff));
        const float yy = rbf(rbf(fin[r]) + rbf(kvv * Dv));
        yb[(t + n) * 2 + ch] = f2bf(yy);
      }
      bar4(bc, 16, lane);
      unsigned short* y = (unsigned short*)(ws + OFF_Y);
#pragma unroll
      for (int i = 0; i < 8; ++i) {
        const int t = tid + 256 * i;
        *(unsigned*)(y + (size_t)t * DM + d0) = ((const unsigned*)yb)[t + (t >> 6)];
      }
      PTS(5);
      TL(4352 + blockIdx.x);
    }
  }
}

DI void gather_mem(rsrc_t rs, unsigned slot, char* lmem, int tid, char* ws) {
  u32x4 v[2];
  unsigned p[2];
#pragma unroll
  for (int r = 0; r < 2; ++r) { int c = tid + 256 * r; if (c >= 448) c = tid; p[r] = slot + c * 16; }
  bool all;
  SPIN_DECL;
  do {
    fence_c();
#pragma unroll
    for (int r = 0; r < 2; ++r) v[r] = ld16(rs, p[r]);
    all = umin(mn4(v[0]), mn4(v[1])) != 0;
    SPIN_CHK(ws, 2);
  } while (!all);
#pragma unroll
  for (int r = 0; r < 2; ++r) {
    const int c = tid + 256 * r;
    if (c < 448) {
      const int vv = c >> 4, row = (c & 15) >> 1, hf = c & 1;
      *(u32x4*)(lmem + row * RS + (vv * 16 + hf * 8) * 2) = v[r];
    }
  }
}

DI void dma_xs(const char* ws, unsigned lxs_off, int l, int s, int wv, int nw, int lane, bool wait) {
  const char* src = ws + (l ? OFF_XS2 : OFF_XS1) + (size_t)s * 32 * DM * 2;
  if (l && wait) wait_cnt((const unsigned*)(ws + OFF_CNT) + (C_XS2 + s) * 32, 16, ws);
  for (int j0 = wv; j0 < 29; j0 += nw) {
    const int j = j0 * 64 + lane;
    int row = j / 57;
    int q = j - row * 57; if (q > 55) q = 55;
    if (row > 31) row = 31;
    dma16(src + row * (DM * 2) + q * 16, lxs_off + j0 * 1024);
  }
}

constexpr int SPS = 464;
DI void role_S(char* ws, char* smem, int h, int l) {
  int tid = threadIdx.x;
  asm volatile("" : "+v"(tid));
  const int lane = tid & 63, w = __builtin_amdgcn_readfirstlane(tid >> 6);
  const int n = lane & 15, g = lane >> 4;
  char* lmem = smem;
  char* lxs = smem + 8 * RS;
  char* lw = lxs + 33 * RS;
  float* red = (float*)(lw + 4 * 14 * 1024);
  char* lsp = (char*)(red + 4 * 16 * 44);
  char* ltr = lsp + 16 * SPS + w * (14 * 384);
  const rsrc_t rs = make_rsrc(ws);
  const char* Af = ws + OFF_AF + ((size_t)h * 28 + 7 * w) * 14 * 1024 + lane * 16;
  bf16x16 Wa[4][7], Wv[2][7];
#pragma unroll
  for (int c = 0; c < 4; ++c) {
#pragma unroll
    for (int kk = 0; kk < 7; ++kk) Wa[c][kk] = cat16(*(const bf16x8*)(Af + (c * 14 + 2 * kk) * 1024), *(const bf16x8*)(Af + (c * 14 + 2 * kk + 1) * 1024));
#pragma unroll
    for (int kk = 0; kk < 7; ++kk) asm volatile("" : "+a"(Wa[c][kk]));
  }
#pragma unroll
  for (int c = 0; c < 2; ++c)
#pragma unroll
    for (int kk = 0; kk < 7; ++kk) Wv[c][kk] = cat16(*(const bf16x8*)(Af + ((4 + c) * 14 + 2 * kk) * 1024), *(const bf16x8*)(Af + ((4 + c) * 14 + 2 * kk + 1) * 1024));
#pragma unroll
  for (int ks = 0; ks < 14; ++ks) *(bf16x8*)(lw + (w * 14 + ks) * 1024 + lane * 16) = *(const bf16x8*)(Af + (6 * 14 + ks) * 1024);
  wait_vm0();
  const unsigned lxo = lds_off(lxs);
  if (l == 0) wait_cnt((const unsigned*)(ws + OFF_CNT) + C_XS1 * 32, 32, ws);
  dma_xs(ws, lxo, l, 0, w, 4, lane, true);
  const char* arow = lsp + n * SPS + 16 * g;
  const int idx = (n & 8) ? 0xEEEE : 0x4444;
  const char* lwp = lw + w * 14 * 1024 + lane * 16;
  char* trw = ltr + (n >> 2) * 384 + (n & 3) * 32 + 8 * g;
  char* trw6 = ltr + (12 + (n >> 3)) * 384 + (n & 7) * 32 + 8 * g;
  const char* trr = ltr + g * 384 + (n >> 2) * 32 + (n & 3) * 8;
  const char* trr6 = ltr + (12 + (g >> 1)) * 384 + (4 * (g & 1) + (n >> 2)) * 32 + (n & 3) * 8;
  unsigned gp[2], gn[2], gs[2];
#pragma unroll
  for (int r = 0; r < 2; ++r) {
    int c = tid + 256 * r; if (c >= 448) c = tid;
    const int vv = c >> 4, row = (c & 15) >> 1, hf = c & 1;
    gp[r] = c * 16; gn[r] = row * RS + (vv * 16 + hf * 8) * 2; gs[r] = row * SPS + (vv * 2 + hf) * 8;
  }
  unsigned tref = 0;
  for (int s = 0; s < NSEG - 1; ++s) {
    const int task = l * NSEG + s;
    if (MDLY && s > 0) delay_until(tref, MDLY);
    {
      const unsigned slot = (unsigned)OFF_MEM + task * MEMB;
      u32x4 v[2];
      bool all;
      SPIN_DECL;
      do {
        fence_c();
#pragma unroll
        for (int r = 0; r < 2; ++r) v[r] = ld16(rs, slot + gp[r]);
        all = umin(mn4(v[0]), mn4(v[1])) != 0;
        SPIN_CHK(ws, 2);
      } while (!all);
#pragma unroll
      for (int r = 0; r < 2; ++r) {
        *(u32x2*)(lsp + gs[r]) = (u32x2){v[r][0], v[r][2]};
        *(u32x2*)(lsp + 8 * SPS + gs[r]) = (u32x2){v[r][1], v[r][3]};
        *(u32x4*)(lmem + gn[r]) = v[r];
      }
    }
    wait_vm0();
    __syncthreads();
    TS((l * 16 + h) * 80 * 4 + s * 4 + 0);
    if ((s & 7) == 0 || s == 62) TL((l * 16 + h) * 16 + (s == 62 ? 8 : s >> 3));
    LT(lt0);
    ST(c0);
    const unsigned nxc = ld4(rs, (unsigned)OFF_CNT + (C_XS2 + s + 1) * 128);
    bf16x8 af[7], kf[3][3];
    bf16x4 kl[3];
#pragma unroll
    for (int kk = 0; kk < 7; ++kk) af[kk] = *(const bf16x8*)(arow + 64 * kk);
    SB();
    auto keyrow = [&](int nt) { return (nt == 0 ? lmem + (n & 7) * RS : lxs + (16 * (nt - 1) + n) * RS) + 224 * w; };
    f32x4 sc[3], t[2];
    bf16x8 tp[3];
    bf16x4 tp3;
    bf16x8 lfh[14];
    auto zero = [&](f32x4& a) { a = (f32x4){0.f, 0.f, 0.f, 0.f}; asm volatile("" : "+v"(a)); };
    auto put = [&](int c) { *(bf16x4*)(c < 6 ? trw + 1536 * (c >> 1) + 128 * (c & 1) : trw6) = pack4(t[c & 1]); };
    auto get = [&](int p) { const bf16x4 lo = ld_tr(trr + 1536 * p), hi = ld_tr(trr + 1536 * p + 128); tp[p] = __builtin_shufflevector(lo, hi, 0, 1, 2, 3, 4, 5, 6, 7); };
    auto ldk = [&](int nt, int p) { kf[nt][p] = *(const bf16x8*)(keyrow(nt) + 64 * p + 16 * g); };
    auto ldl = [&](int i) { lfh[i] = *(const bf16x8*)(lwp + i * 1024); };
    auto score = [&](int nt, int p) { sc[nt] = mfma32(kf[nt][p], tp[p], p ? sc[nt] : (f32x4){0.f, 0.f, 0.f, 0.f}); asm volatile("" : "+a"(sc[nt])); };
    zero(t[0]); zero(t[1]);
    SB();
#pragma unroll
    for (int c = 0; c < 7; ++c)
#pragma unroll
      for (int kk = 0; kk < 7; ++kk) {
        t[c & 1] = smfma(af[kk], c < 4 ? Wa[c < 4 ? c : 0][kk] : c < 6 ? Wv[c < 6 && c >= 4 ? c - 4 : 0][kk] : cat16(lfh[2 * kk], lfh[2 * kk + 1]), t[c & 1], idx);
        asm volatile("" : "+v"(t[c & 1]));
        SB();
        if (c == 0 && kk >= 1 && kk < 4) ldk(kk - 1, 0);
        if (c == 2 && kk < 3) ldk(kk, 1);
        if (c == 4 && kk < 4) { ldl(2 * kk); ldl(2 * kk + 1); }
        if (c == 5 && kk < 3) { ldl(8 + 2 * kk); ldl(9 + 2 * kk); }
        if (c == 6 && kk < 3) { ldk(kk, 2); kl[kk] = *(const bf16x4*)(keyrow(kk) + 192 + 8 * g); }
        if (c >= 1 && kk == 4) put(c - 1);
        if (c >= 1 && c < 6 && kk == 5) zero(t[(c - 1) & 1]);
        if ((c == 2 || c == 4 || c == 6) && kk == 5) get(c / 2 - 1);
        if ((c == 3 || c == 5) && kk >= 3 && kk < 6) score(kk - 3, (c - 3) / 2);
        SB();
      }
    ST(c1);
    put(6);
    tp3 = ld_tr(trr6);
    SB();
#pragma unroll
    for (int nt = 0; nt < 3; ++nt) score(nt, 2);
    SB();
#pragma unroll
    for (int nt = 0; nt < 3; ++nt) sc[nt] = mfma16(kl[nt], tp3, sc[nt]);
    ST(c2);
    {
      float* rw = red + (w * 16 + n) * 44;
      *(f32x4*)(rw + 4 * g) = sc[1];
      *(f32x4*)(rw + 16 + 4 * g) = sc[2];
      if (g < 2) *(f32x4*)(rw + 32 + 4 * g) = sc[0];
    }
    __syncthreads();
    ST(c3);
    const bool pre = l == 0 || __builtin_amdgcn_readfirstlane((int)(nxc >= 16u)) != 0;
    {
      const int r = 2 * w + (g & 1), jj = n < 10 ? n : 9;
      f32x4 sv = *(const f32x4*)(red + r * 44 + 4 * jj);
#pragma unroll
      for (int ww = 1; ww < 8; ++ww) sv += *(const f32x4*)(red + (ww * 8 + r) * 44 + 4 * jj);
      sv *= 0.0625f;
      float mx = fmaxf(fmaxf(sv[0], sv[1]), fmaxf(sv[2], sv[3]));
      mx = max_ror16<8>(mx); mx = max_ror16<4>(mx); mx = max_ror16<2>(mx); mx = max_ror16<1>(mx);
#pragma unroll
      for (int i = 0; i < 4; ++i) sv[i] = __expf(sv[i] - mx);
      float sum = n < 10 ? (sv[0] + sv[1]) + (sv[2] + sv[3]) : 0.f;
      sum += ror16<8>(sum); sum += ror16<4>(sum); sum += ror16<2>(sum); sum += ror16<1>(sum);
      const float inv = __builtin_amdgcn_rcpf(sum);
      const unsigned pb = (unsigned)OFF_PB + (task * NH + h) * PBB;
      const u32x2 pv = {nz(pk2(sv[0] * inv, sv[1] * inv)), nz(pk2(sv[2] * inv, sv[3] * inv))};
      if (n < 10 && g < 2) st8(rs, pb + r * 80 + (n < 8 ? (n & 3) * 16 + (n >> 2) * 8 : 64 + (n - 8) * 8), pv);
    }
    tref = rtime();
    {
      ST(c4);
      TS((l * 16 + h) * 80 * 4 + s * 4 + 1);
      STS((l * 16 + h) * 80 * 4 + s * 4 + 0, c1 - c0); STS((l * 16 + h) * 80 * 4 + s * 4 + 1, c2 - c1); STS((l * 16 + h) * 80 * 4 + s * 4 + 2, c3 - c2); STS((l * 16 + h) * 80 * 4 + s * 4 + 3, (unsigned long long)(pre ? 0 : 1));
    }
    LT(lt1);
    if (s == 0 && l == 0) wait_cnt((const unsigned*)(ws + OFF_CNT) + C_LNALL * 32, NAW * 16, ws);
    if (s + 2 < NSEG) dma_xs(ws, lxo, l, s + 1, w, 4, lane, !pre);
    LW(s * 4 + 0, lt0); LW(s * 4 + 1, lt1);
  }
  LDUMP((l * 16 + h) * 80 * 4);
}

DI void role_V(char* ws, char* smem, int v, int l, const unsigned short* mem0) {
  int tid = threadIdx.x;
  asm volatile("" : "+v"(tid));
  const int lane = tid & 63, w = __builtin_amdgcn_readfirstlane(tid >> 6);
  const int n = lane & 15, g = lane >> 4;
  char* lmem = smem;
  float* red = (float*)(smem + 8 * RS);
  const rsrc_t rs = make_rsrc(ws);
  unsigned* cnt = (unsigned*)(ws + OFF_CNT);
  bf16x8 W[4][14];
#pragma unroll
  for (int hh = 0; hh < 4; ++hh)
#pragma unroll
    for (int ks = 0; ks < 14; ++ks) lda(W[hh][ks], ws + OFF_BF + (((size_t)(4 * w + hh) * 28 + v) * 14 + ks) * 1024 + lane * 16);
  if (tid < 64 && n < 8) {
    const u32x2 m0v = nz2(*(const u32x2*)(mem0 + (size_t)n * DM + v * 16 + g * 4));
    st8(rs, (unsigned)OFF_MEM + l * NSEG * MEMB + v * 256 + n * 32 + g * 8, m0v);
  }
  wait_vm0();
#pragma unroll
  for (int hh = 0; hh < 4; ++hh)
#pragma unroll
    for (int ks = 0; ks < 14; ++ks) asm volatile("" : "+a"(W[hh][ks]));
  unsigned po[5];
#pragma unroll
  for (int j = 0; j < 4; ++j) po[j] = (4 * w + j) * PBB + (n & 7) * 80 + g * 16;
  po[4] = (4 * w + g) * PBB + (n & 7) * 80 + 64;
  const unsigned wxo = (4 * w * 28 + v) * 1024 + lane * 16;
  u32x4 Wn[4];
  auto load_wx = [&](int task) {
    fence_c();
#pragma unroll
    for (int j = 0; j < 4; ++j) Wn[j] = ld16(rs, (unsigned)OFF_WX + task * (NH * 28 * 1024) + wxo + j * (28 * 1024));
  };
  bool have = false;
  unsigned tref = 0;
  unsigned long long nhv = 0;
  const char* mrow = lmem + (n & 7) * RS + 16 * g;
  for (int s = 0; s < NSEG; ++s) {
    const int task = l * NSEG + s;
    if (VMD && s > 0) delay_until(tref, VMD);
    gather_mem(rs, (unsigned)OFF_MEM + task * MEMB, lmem, tid, ws);
    __syncthreads();
    const unsigned tref0 = rtime();
    TS((32 + l * 28 + v) * 80 * 4 + s * 4 + 0);
    LT(lt0);
    ST(c0);
    const unsigned nc = ld4(rs, (unsigned)OFF_CNT + (C_WX + (s + 1 < NSEG ? task + 1 : task)) * 128);
    SB();
    u32x2 Wm[4];
    {
      bf16x8 mf[14];
#pragma unroll
      for (int ks = 0; ks < 14; ++ks) mf[ks] = *(const bf16x8*)(mrow + 64 * ks);
      SB();
      f32x4 a[4];
#pragma unroll
      for (int hh = 0; hh < 4; ++hh) a[hh] = (f32x4){0.f, 0.f, 0.f, 0.f};
#pragma unroll
      for (int ks = 0; ks < 14; ++ks)
#pragma unroll
        for (int hh = 0; hh < 4; ++hh) a[hh] = mfma32(mf[ks], W[hh][ks], a[hh]);
#pragma unroll
      for (int hh = 0; hh < 4; ++hh) {
        Wm[hh] = __builtin_bit_cast(u32x2, pack4(a[hh]));
        if (g < 2 || s + 1 < NSEG)
          st8(rs, (unsigned)OFF_WM + (g < 2 ? 0 : SZ_WM1) + ((task * NH + 4 * w + hh) * 28 + v) * 256 + (g & 1) * 128 + n * 8, g < 2 ? nz2(Wm[hh]) : (u32x2){0u, 0u});
      }
    }
    if (s == NSEG - 1) break;
    if (!have) {
      nhv |= 1ull << s;
      wait_cnt(cnt + (C_WX + task) * 32, 64, ws);
      load_wx(task);
    }
    u32x4 Wo[5];
    {
      const u32x2 keep = g == 0 ? Wm[0] : g == 1 ? Wm[1] : g == 2 ? Wm[2] : Wm[3];
      const u32x2 send = g == 0 ? Wm[1] : g == 1 ? Wm[0] : g == 2 ? Wm[3] : Wm[2];
      const u32x2 recv = {(unsigned)__shfl_xor((int)send[0], 16), (unsigned)__shfl_xor((int)send[1], 16)};
      const u32x4 full = (g & 1) ? (u32x4){recv[0], recv[1], keep[0], keep[1]} : (u32x4){keep[0], keep[1], recv[0], recv[1]};
#pragma unroll
      for (int j = 0; j < 4; ++j) Wo[j] = Wn[j];
      Wo[4] = full;
#pragma unroll
      for (int j = 0; j < 5; ++j) asm volatile("" : "+v"(Wo[j]));
    }
    ST(c1);
    u32x4 px[5];
    {
      const unsigned pb = (unsigned)OFF_PB + task * (NH * PBB);
      if (PDLY && s > 0) delay_until(tref0, PDLY);
      bool all;
      SPIN_DECL;
      do {
        fence_c();
#pragma unroll
        for (int j = 0; j < 5; ++j) px[j] = ld16(rs, pb + po[j]);
        all = umin(umin(umin(mn4(px[0]), mn4(px[1])), umin(mn4(px[2]), mn4(px[3]))), mn4(px[4])) != 0;
        SPIN_CHK(ws, 3);
      } while (!all);
    }
    TS((32 + l * 28 + v) * 80 * 4 + s * 4 + 1);
    LT(lt1);
    {
      f32x4 o1 = {0.f, 0.f, 0.f, 0.f}, o2 = o1;
      o1 = mfma32(__builtin_bit_cast(bf16x8, Wo[0]), __builtin_bit_cast(bf16x8, px[0]), o1);
      o2 = mfma32(__builtin_bit_cast(bf16x8, Wo[1]), __builtin_bit_cast(bf16x8, px[1]), o2);
      o1 = mfma32(__builtin_bit_cast(bf16x8, Wo[2]), __builtin_bit_cast(bf16x8, px[2]), o1);
      o2 = mfma32(__builtin_bit_cast(bf16x8, Wo[3]), __builtin_bit_cast(bf16x8, px[3]), o2);
      o1 = mfma32(__builtin_bit_cast(bf16x8, Wo[4]), __builtin_bit_cast(bf16x8, px[4]), o1);
      if (n < 8) *(f32x4*)(red + (w * 8 + n) * 16 + 4 * g) = o1 + o2;
    }
    ST(c3);
    wait_vm0();
    __syncthreads();
    tref = rtime();
    ST(c4);
    if (tid < 64) {
      if (n < 8) {
        f32x4 s0 = *(const f32x4*)(red + n * 16 + 4 * g);
#pragma unroll
        for (int ww = 1; ww < 4; ++ww) s0 += *(const f32x4*)(red + (ww * 8 + n) * 16 + 4 * g);
        const u32x2 m = nz2((u32x2){pk2(s0[0], s0[1]), pk2(s0[2], s0[3])});
        st8(rs, (unsigned)OFF_MEM + (task + 1) * MEMB + v * 256 + n * 32 + g * 8, m);
      }
    } else if (tid == 64) {
      add_cnt(cnt + (C_WM + task) * 32);
    }
    ST(c5);
    TS((32 + l * 28 + v) * 80 * 4 + s * 4 + 2);
    LT(lt2);
    STS((32 + l * 28 + v) * 80 * 4 + s * 4 + 0, c1 - c0); STS((32 + l * 28 + v) * 80 * 4 + s * 4 + 1, c3 - c1); STS((32 + l * 28 + v) * 80 * 4 + s * 4 + 2, c4 - c3); STS((32 + l * 28 + v) * 80 * 4 + s * 4 + 3, (unsigned long long)(have ? 0 : 1));
    have = s + 2 < NSEG && __builtin_amdgcn_readfirstlane((int)(nc >= 64u)) != 0;
    __syncthreads();
    if (have) load_wx(task + 1);
    TS((32 + l * 28 + v) * 80 * 4 + s * 4 + 3);
    LW(s * 4 + 0, lt0); LW(s * 4 + 1, lt1); LW(s * 4 + 2, lt2); LW(s * 4 + 3, (unsigned long long)(have ? 0 : 1));
  }
  TLV(1024 + l * 28 + v, nhv);
  LDUMP((32 + l * 28 + v) * 80 * 4);
}

DI void load_x(rsrc_t rs, char* lh, unsigned src, int tid) {
  u32x4 v[7];
#pragma unroll
  for (int r = 0; r < 7; ++r) { const int c = tid + 256 * r; v[r] = ld16(rs, src + (c / 56) * DM * 2 + (c % 56) * 16); }
#pragma unroll
  for (int r = 0; r < 7; ++r) { const int c = tid + 256 * r; *(u32x4*)(lh + (c / 56) * RS + (c % 56) * 16) = v[r]; }
}

constexpr int NFA = 50, NFV = 27;
#define KSH 0
#define KHA 1
#define KHOOK 9
template <bool TR, class F>
DI void gemm_res(const bf16x8 (&Wa)[NFA], const bf16x8 (&Wv)[NFV], const char* lwp, const char* xrow, f32x4 (&acc)[2][7], F hook) {
#pragma unroll
  for (int mt = 0; mt < 2; ++mt)
#pragma unroll
    for (int c = 0; c < 7; ++c) acc[mt][c] = (f32x4){0.f, 0.f, 0.f, 0.f};
  bf16x8 x0[2], x1[2];
  x0[0] = *(const bf16x8*)(xrow); x1[0] = *(const bf16x8*)(xrow + 16 * RS);
  SB();
#pragma unroll
  for (int ks = 0; ks < 14; ++ks) {
    const int cu = ks & 1, nx = cu ^ 1;
    const bf16x8 lf = *(const bf16x8*)(lwp + ks * 1024);
    bf16x8 lg = lf;
    if (ks >= 7) lg = *(const bf16x8*)(lwp + (ks + 7) * 1024);
    if (ks + 1 < 14) { x0[nx] = *(const bf16x8*)(xrow + 64 * (ks + 1)); x1[nx] = *(const bf16x8*)(xrow + 16 * RS + 64 * (ks + 1)); }
    SB();
#pragma unroll
    for (int c = 0; c < 7; ++c) {
      const int f = c * 14 + ks;
      const bf16x8 wf = f < NFA ? Wa[f < NFA ? f : 0] : f < NFA + NFV ? Wv[f < NFA + NFV ? f - NFA : 0] : c == 5 ? lg : lf;
      acc[0][c] = TR ? mfma32(wf, x0[cu], acc[0][c]) : mfma32(x0[cu], wf, acc[0][c]);
      acc[1][c] = TR ? mfma32(wf, x1[cu], acc[1][c]) : mfma32(x1[cu], wf, acc[1][c]);
      if (c == 4) SB();
    }
    SB();
    if (ks == KHOOK || ks == KHA) { hook(ks); SB(); }
  }
}
DI float wave_sum_dpp(float v) {
  v += ror16<8>(v); v += ror16<4>(v); v += ror16<2>(v); v += ror16<1>(v);
  const int u = __builtin_bit_cast(int, v);
  const float a = __builtin_bit_cast(float, __builtin_amdgcn_readlane(u, 0)), b = __builtin_bit_cast(float, __builtin_amdgcn_readlane(u, 16));
  const float c = __builtin_bit_cast(float, __builtin_amdgcn_readlane(u, 32)), d = __builtin_bit_cast(float, __builtin_amdgcn_readlane(u, 48));
  return (a + b) + (c + d);
}

DI void role_W(char* ws, char* smem, int j, int h, const unsigned short* xin, unsigned short* out) {
  int tid = threadIdx.x;
  asm volatile("" : "+v"(tid));
  const int lane = tid & 63, w = __builtin_amdgcn_readfirstlane(tid >> 6);
  const int n = lane & 15, g = lane >> 4;
  char* lh = smem;
  float* red = (float*)(smem + 48 * RS);
  char* lp = (char*)(red + 4 * 32 * 48);
  char* lw = lp + 32 * 112;
  unsigned* cnt = (unsigned*)(ws + OFF_CNT);
  const rsrc_t rs = make_rsrc(ws);
  if (j >= NWXT) {
    const bool act = lane < 48;
    const int c0 = 4 * lane, c1 = act ? 256 + 4 * lane : c0;
    const int r0 = ((j - NWXT) * 16 + h) * 4 + w;
    constexpr int NP = (SEQ + NAW * 64 - 1) / (NAW * 64);
    u32x2 xr[NP][2], yr[NP][2];
#pragma unroll
    for (int p = 0; p < NP; ++p) {
      const int row = r0 + p * (NAW * 64) < SEQ ? r0 + p * (NAW * 64) : r0;
      const char* px = (const char*)xin + (size_t)row * DM * 2;
      const char* py = ws + OFF_Y + (size_t)row * DM * 2;
      xr[p][0] = *(const u32x2*)(px + c0 * 2); xr[p][1] = *(const u32x2*)(px + c1 * 2);
      yr[p][0] = *(const u32x2*)(py + c0 * 2); yr[p][1] = *(const u32x2*)(py + c1 * 2);
    }
#pragma unroll
    for (int p = 0; p < NP; ++p) {
      const int row = r0 + p * (NAW * 64);
      if (row < SEQ) {
        float xv[2][4];
#pragma unroll
        for (int q = 0; q < 2; ++q)
#pragma unroll
          for (int i = 0; i < 4; ++i) { const unsigned u = xr[p][q][i >> 1]; xv[q][i] = bf2f((unsigned short)((i & 1) ? u >> 16 : u & 0xffff)); }
#pragma unroll
        for (int pass = 0; pass < 2; ++pass) {
          const float s0 = (xv[0][0] + xv[0][1]) + (xv[0][2] + xv[0][3]), s1 = (xv[1][0] + xv[1][1]) + (xv[1][2] + xv[1][3]);
          const float mean = wave_sum_dpp(act ? s0 + s1 : s0) * (1.f / DM);
          float q0 = 0.f, q1 = 0.f;
#pragma unroll
          for (int i = 0; i < 4; ++i) { const float d0 = xv[0][i] - mean, d1 = xv[1][i] - mean; q0 += d0 * d0; q1 += d1 * d1; }
          const float var = wave_sum_dpp(act ? q0 + q1 : q0) * (1.f / DM);
          const float mb = rbf(mean), den = rbf(sqrtf(rbf(rbf(var) + 1e-5f)));
#pragma unroll
          for (int q = 0; q < 2; ++q)
#pragma unroll
            for (int i = 0; i < 4; ++i) {
              const float ln = rbf(rbf(xv[q][i] - mb) / den);
              const unsigned u = yr[p][q][i >> 1];
              xv[q][i] = pass == 0 ? rbf(xv[q][i] + rbf(ln * bf2f((unsigned short)((i & 1) ? u >> 16 : u & 0xffff)))) : ln;
            }
          const unsigned dst = (unsigned)(pass == 0 ? OFF_X1 : OFF_XS1) + (unsigned)row * DM * 2;
          st8(rs, dst + c0 * 2, (u32x2){pk2(xv[0][0], xv[0][1]), pk2(xv[0][2], xv[0][3])});
          if (act) st8(rs, dst + c1 * 2, (u32x2){pk2(xv[1][0], xv[1][1]), pk2(xv[1][2], xv[1][3])});
        }
        wait_vm0();
        if (lane == 0) add_cnt(cnt + (C_XS1 + (row >> 5)) * 32);
      }
    }
    __syncthreads();
    if (tid == 0) add_cnt(cnt + C_LNALL * 32);
  }
  for (int i = tid; i < 8 * 57; i += 256) *(u32x4*)(lh + 40 * RS + i * 16) = (u32x4){0u, 0u, 0u, 0u};
  const char* Wf = ws + (j < NWXT ? OFF_BF : OFF_AF) + ((size_t)h * 28 + 7 * w) * 14 * 1024 + lane * 16;
  bf16x8 Wa[NFA], Wv[NFV];
#pragma unroll
  for (int ks = 0; ks < 14; ++ks) *(bf16x8*)(lw + (w * 21 + ks) * 1024 + lane * 16) = *(const bf16x8*)(Wf + (6 * 14 + ks) * 1024);
#pragma unroll
  for (int ks = 0; ks < 7; ++ks) *(bf16x8*)(lw + (w * 21 + 14 + ks) * 1024 + lane * 16) = *(const bf16x8*)(Wf + (5 * 14 + 7 + ks) * 1024);
  fence_c();
#pragma unroll
  for (int k = 0; k < NFA; ++k) lda(Wa[k], Wf + k * 1024);
#pragma unroll
  for (int k = 0; k < NFV; ++k) Wv[k] = *(const bf16x8*)(Wf + (NFA + k) * 1024);
  wait_vm0();
#pragma unroll
  for (int k = 0; k < NFA; ++k) asm volatile("" : "+a"(Wa[k]));
  if (j < NWXT) {
    static_assert(NWX1 == 1, "unit order");
    auto nextu = [](int u) { return u < 2 * KSH - 1 ? u + 2 : u == 2 * KSH - 1 ? 64 : u + 1; };
    auto cidx = [](int u) { return (unsigned)(u >> 6 ? C_XS2 + (u & 63) : C_XS1 + u); };
    auto thr = [](int u) { return u >> 6 ? 16u : 32u; };
    const int uend = j ? 128 : 64;
    const unsigned lx0 = lds_off(lh);
    int u = j ? (KSH ? 1 : 64) : 0;
    wait_cnt_wg<WXSL>(cnt + cidx(u) * 32, thr(u), ws);
    dma_xs(ws, lx0, u >> 6, u & 63, w, 4, threadIdx.x & 63, false);
    int pend = -1, bsel = 0;
    bool rdy = __builtin_amdgcn_readfirstlane((int)(ld4(rs, (unsigned)OFF_CNT + cidx(nextu(u)) * 128) >= thr(nextu(u)))) != 0;
    fence_c();
    for (; u < uend; u = nextu(u)) {
      const int task = u, un = nextu(u), un2 = nextu(un);
      asm volatile("" : "+v"(tid));
      const int lane = tid & 63, n = lane & 15, g = lane >> 4;
      const char* lwp = lw + w * 21 * 1024 + lane * 16;
      const char* xrow = lh + bsel * (33 * RS) + n * RS + 16 * g;
      const int tsb = 88 * 80 * 4 + (task * 16 + h) * 8;
      TSW(tsb + 0);
      if (pend >= 0) asm volatile("s_waitcnt vmcnt(7)" ::: "memory"); else wait_vm0();
      __syncthreads();
      TSW(tsb + 1);
      if (h == 0) TL(6144 + task * 4 + 0);
      if (rdy) dma_xs(ws, lx0 + (bsel ^ 1) * (33 * RS), un >> 6, un & 63, w, 4, lane, false);
      unsigned nc = 0;
      f32x4 acc[2][7];
      gemm_res<false>(Wa, Wv, lwp, xrow, acc, [&](int ks) {
        if (ks == KHA) {
          if (pend >= 0) {
            if (rdy) asm volatile("s_waitcnt vmcnt(7)" ::: "memory"); else wait_vm0();
            if (lane == 0) add_cnt(cnt + (C_WX + pend) * 32);
          }
        } else nc = ld4(rs, (unsigned)OFF_CNT + cidx(un2 < uend ? un2 : u) * 128);
      });
      const unsigned wxo = (unsigned)OFF_WX + ((task * NH + h) * 28 + 7 * w) * 1024 + lane * 16;
#pragma unroll
      for (int c = 0; c < 7; ++c) st16(rs, wxo + c * 1024, __builtin_bit_cast(u32x4, pack8(acc[0][c], acc[1][c])));
      fence_c();
      if (h == 0) { TL(6144 + task * 4 + 1); TLV(6144 + task * 4 + 2, rdy ? 1ull : 0ull); }
      if (rdy) {
        pend = u;
        rdy = un2 < uend && __builtin_amdgcn_readfirstlane((int)(nc >= thr(un2))) != 0;
      } else {
        wait_vm0();
        if (lane == 0) add_cnt(cnt + (C_WX + task) * 32);
        pend = -1;
        if (un < uend) {
          wait_cnt(cnt + cidx(un) * 32, thr(un), ws);
          dma_xs(ws, lx0 + (bsel ^ 1) * (33 * RS), un >> 6, un & 63, w, 4, lane, false);
          rdy = un2 < uend && __builtin_amdgcn_readfirstlane((int)(nc >= thr(un2))) != 0;
        }
      }
      bsel ^= 1;
      TSW(tsb + 2);
    }
    if (pend >= 0) {
      wait_vm0();
      if ((threadIdx.x & 63) == 0) add_cnt(cnt + (C_WX + pend) * 32);
    }
    return;
  }
  {
    const int l = j - NWXT >= NAW0;
    for (int s = l ? j - NWXT - NAW0 : j - NWXT; s < NSEG; s += l ? NAW1 : NAW0) {
      const int task = l * 64 + s;
      asm volatile("" : "+v"(tid));
      const int lane = tid & 63, n = lane & 15, g = lane >> 4;
      const char* lwp = lw + w * 21 * 1024 + lane * 16;
      const char* xrow = lh + n * RS + 16 * g;
      __syncthreads();
      const int tsb = 88 * 80 * 4 + (128 * 16 + task * 16 + h) * 8;
      TSW(tsb + 0);
      if (h == 0) TL(3072 + task * 4 + 0);
      wait_cnt_wg(cnt + (l == 1 ? C_XS2 + s : C_XS1 + s) * 32, l == 1 ? 16 : 32, ws);
      fence_c();
      load_x(rs, lh, (unsigned)(l == 0 ? OFF_XS1 : OFF_XS2) + s * 32 * DM * 2, tid);
      __syncthreads();
      TSW(tsb + 1);
      bf16x8 tA[2][3]; bf16x4 tB[2];
      {
        f32x4 acc[2][7];
        gemm_res<true>(Wa, Wv, lwp, xrow, acc, [](int) {});
#pragma unroll
        for (int mt = 0; mt < 2; ++mt) {
#pragma unroll
          for (int p = 0; p < 3; ++p) tA[mt][p] = pack8(acc[mt][2 * p], acc[mt][2 * p + 1]);
          tB[mt] = pack4(acc[mt][6]);
        }
      }
      TSW(tsb + 2);
      u32x4 wx[7]; u32x2 wm[7];
      wait_cnt_wg(cnt + (C_WX + task) * 32, 64, ws);
      fence_c();
#pragma unroll
      for (int c = 0; c < 7; ++c) wx[c] = ld16(rs, (unsigned)OFF_WX + ((task * NH + h) * 28 + 7 * w + c) * 1024 + lane * 16);
      if (s > 0) wait_cnt_wg(cnt + (C_WM + task - 1) * 32, NV, ws);
      {
        const unsigned slot = (unsigned)OFF_MEM + task * MEMB;
        u32x4 v[2]; unsigned p[2];
#pragma unroll
        for (int r = 0; r < 2; ++r) { int c = tid + 256 * r; if (c >= 448) c = tid; p[r] = slot + c * 16; }
        bool all;
        SPIN_DECL;
        if (MSLP0) __builtin_amdgcn_s_sleep(MSLP0);
        do {
          fence_c();
#pragma unroll
          for (int r = 0; r < 2; ++r) v[r] = ld16(rs, p[r]);
          all = (zc4(v[0]) | zc4(v[1])) == 0;
          if (!all) __builtin_amdgcn_s_sleep(MSLP);
          SPIN_CHK(ws, 4);
        } while (!all);
#pragma unroll
        for (int r = 0; r < 2; ++r) {
          const int c = tid + 256 * r;
          if (c < 448) { const int vv = c >> 4, row = (c & 15) >> 1, hf = c & 1; *(u32x4*)(lh + (32 + row) * RS + (vv * 16 + hf * 8) * 2) = v[r]; }
        }
      }
      __syncthreads();
      TSW(tsb + 3);
      if (h == 0) TL(3072 + task * 4 + 1);
      {
        f32x4 sacc[2][3];
#pragma unroll
        for (int nt = 0; nt < 3; ++nt) {
          const char* rowp = lh + (16 * nt + n) * RS + 224 * w;
          bf16x8 kf[3];
#pragma unroll
          for (int p = 0; p < 3; ++p) kf[p] = *(const bf16x8*)(rowp + 64 * p + 16 * g);
          const bf16x4 kl = *(const bf16x4*)(rowp + 192 + 8 * g);
#pragma unroll
          for (int mt = 0; mt < 2; ++mt) {
            f32x4 sa = {0.f, 0.f, 0.f, 0.f};
#pragma unroll
            for (int p = 0; p < 3; ++p) sa = mfma32(tA[mt][p], kf[p], sa);
            sacc[mt][nt] = mfma16(tB[mt], kl, sa);
          }
        }
#pragma unroll
        for (int mt = 0; mt < 2; ++mt)
#pragma unroll
          for (int nt = 0; nt < 3; ++nt)
#pragma unroll
            for (int i = 0; i < 4; ++i) red[(w * 32 + 16 * mt + 4 * g + i) * 48 + 16 * nt + n] = sacc[mt][nt][i];
      }
      __syncthreads();
      {
        const int r = tid >> 3, q = tid & 7;
        float sv[5]; float mx = -3.0e38f;
#pragma unroll
        for (int i = 0; i < 5; ++i) {
          const int k = q * 5 + i;
          float a = red[(0 * 32 + r) * 48 + k] + red[(1 * 32 + r) * 48 + k] + red[(2 * 32 + r) * 48 + k] + red[(3 * 32 + r) * 48 + k];
          a *= 0.0625f;
          const bool ok = (k >= 32) || (k <= r);
          sv[i] = ok ? a : -3.0e38f;
          mx = fmaxf(mx, sv[i]);
        }
        mx = fmaxf(mx, __shfl_xor(mx, 1)); mx = fmaxf(mx, __shfl_xor(mx, 2)); mx = fmaxf(mx, __shfl_xor(mx, 4));
        float sum = 0.f;
#pragma unroll
        for (int i = 0; i < 5; ++i) { sv[i] = sv[i] > -1.0e38f ? __expf(sv[i] - mx) : 0.f; sum += sv[i]; }
        sum += __shfl_xor(sum, 1); sum += __shfl_xor(sum, 2); sum += __shfl_xor(sum, 4);
        const float inv = 1.f / sum;
#pragma unroll
        for (int i = 0; i < 5; ++i) { const int k = q * 5 + i; *(unsigned short*)(lp + r * 112 + k * 2) = f2bf(sv[i] * inv); }
        if (q == 0) { u32x4 z = {0u, 0u, 0u, 0u}; asm volatile("" : "+v"(z)); *(u32x4*)(lp + r * 112 + 80) = z; }
      }
      __syncthreads();
      TSW(tsb + 4);
      {
        bool all;
        SPIN_DECL;
        for (;;) {
          fence_c();
#pragma unroll
          for (int c = 0; c < 7; ++c) wm[c] = ld8(rs, (unsigned)OFF_WM + ((task * NH + h) * 28 + 7 * w + c) * 256 + (g & 1) * 128 + n * 8);
          unsigned mn = ~0u;
#pragma unroll
          for (int c = 0; c < 7; ++c) mn = umin(mn, umin(wm[c][0], wm[c][1]));
          all = mn != 0;
          if (all) break;
          __builtin_amdgcn_s_sleep(WSLP);
          SPIN_CHK(ws, 5);
        }
      }
      {
        const unsigned part = (unsigned)OFF_PART + (task * NH + h) * (unsigned)SZ_PART1;
#pragma unroll
        for (int mt = 0; mt < 2; ++mt) {
          const char* pr = lp + (16 * mt + n) * 112;
          u32x2 lo = *(const u32x2*)(pr + 8 * g), hi = *(const u32x2*)(pr + 32 + 8 * g);
          const bf16x8 pf = __builtin_bit_cast(bf16x8, (u32x4){lo[0], lo[1], hi[0], hi[1]});
          const bf16x4 pmf = __builtin_bit_cast(bf16x4, *(const u32x2*)(pr + 64 + 8 * g));
#pragma unroll
          for (int c = 0; c < 7; ++c) {
            f32x4 o = {0.f, 0.f, 0.f, 0.f};
            o = mfma32(__builtin_bit_cast(bf16x8, wx[c]), pf, o);
            o = mfma16(__builtin_bit_cast(bf16x4, wm[c]), pmf, o);
            st16(rs, part + (16 * mt + n) * (DM * 4) + (16 * (7 * w + c) + 4 * g) * 4, __builtin_bit_cast(u32x4, o));
          }
        }
      }
      const int row = 2 * h + (w >> 1), hb = w & 1;
      const bool act = lane < 48;
      const int c0 = 4 * lane, c1 = act ? 256 + 4 * lane : c0;
      const unsigned rbase = (unsigned)((32 * s + row) * DM) * 2;
      u32x2 xr[2], yr[2];
      if (hb == 0) {
        const unsigned xo = (unsigned)(l == 0 ? OFF_X1 : OFF_X2) + rbase;
        xr[0] = ld8(rs, xo + c0 * 2); xr[1] = ld8(rs, xo + c1 * 2);
        if (l == 0) { yr[0] = *(const u32x2*)(ws + OFF_Y + rbase + c0 * 2); yr[1] = *(const u32x2*)(ws + OFF_Y + rbase + c1 * 2); }
        else { yr[0] = (u32x2){0u, 0u}; yr[1] = yr[0]; }
      }
      wait_vm0();
      __syncthreads();
      if (tid == 0) add_cnt(cnt + (C_PART + task) * 32);
      TSW(tsb + 5);
      {
        wait_cnt_wg<PSLP>(cnt + (C_PART + task) * 32, 16, ws);
        fence_c();
        TSW(tsb + 6);
        f32x4 a[2] = {{0.f, 0.f, 0.f, 0.f}, {0.f, 0.f, 0.f, 0.f}};
        const unsigned p = (unsigned)OFF_PART + task * NH * (unsigned)SZ_PART1 + row * (DM * 4);
        {
          u32x4 v[8][2];
#pragma unroll
          for (int hh = 0; hh < 8; ++hh) { v[hh][0] = ld16(rs, p + (8 * hb + hh) * (unsigned)SZ_PART1 + c0 * 4); v[hh][1] = ld16(rs, p + (8 * hb + hh) * (unsigned)SZ_PART1 + c1 * 4); }
#pragma unroll
          for (int hh = 0; hh < 8; ++hh) { a[0] += __builtin_bit_cast(f32x4, v[hh][0]); a[1] += __builtin_bit_cast(f32x4, v[hh][1]); }
        }
        {
          f32x4* ex = (f32x4*)red + (w >> 1) * 128 + lane;
          if (hb) { ex[0] = a[0]; ex[64] = a[1]; }
          __syncthreads();
          if (!hb) { a[0] += ex[0]; a[1] += ex[64]; }
        }
        if (!hb) {
        float xv[2][4];
#pragma unroll
        for (int q = 0; q < 2; ++q)
#pragma unroll
          for (int i = 0; i < 4; ++i) {
            const unsigned u = xr[q][i >> 1];
            xv[q][i] = rbf(bf2f((unsigned short)((i & 1) ? u >> 16 : u & 0xffff)) + rbf(a[q][i]));
          }
        if (l == 1) {
          *(u32x2*)((char*)out + rbase + c0 * 2) = (u32x2){pk2(xv[0][0], xv[0][1]), pk2(xv[0][2], xv[0][3])};
          if (act) *(u32x2*)((char*)out + rbase + c1 * 2) = (u32x2){pk2(xv[1][0], xv[1][1]), pk2(xv[1][2], xv[1][3])};
        } else {
#pragma unroll
          for (int pass = 0; pass < 2; ++pass) {
            const float s0 = (xv[0][0] + xv[0][1]) + (xv[0][2] + xv[0][3]), s1 = (xv[1][0] + xv[1][1]) + (xv[1][2] + xv[1][3]);
            const float mean = wave_sum_dpp(act ? s0 + s1 : s0) * (1.f / DM);
            float q0 = 0.f, q1 = 0.f;
#pragma unroll
            for (int i = 0; i < 4; ++i) { const float d0 = xv[0][i] - mean, d1 = xv[1][i] - mean; q0 += d0 * d0; q1 += d1 * d1; }
            const float var = wave_sum_dpp(act ? q0 + q1 : q0) * (1.f / DM);
            const float mb = rbf(mean), den = rbf(sqrtf(rbf(rbf(var) + 1e-5f)));
            float ln[2][4];
#pragma unroll
            for (int q = 0; q < 2; ++q)
#pragma unroll
              for (int i = 0; i < 4; ++i) {
                ln[q][i] = rbf(rbf(xv[q][i] - mb) / den);
                if (pass == 0) {
                  const unsigned u = yr[q][i >> 1];
                  xv[q][i] = rbf(xv[q][i] + rbf(ln[q][i] * bf2f((unsigned short)((i & 1) ? u >> 16 : u & 0xffff))));
                }
              }
            const unsigned dst = (unsigned)(pass == 0 ? OFF_X2 : OFF_XS2) + rbase;
            if (pass == 0) {
              st8(rs, dst + c0 * 2, (u32x2){pk2(xv[0][0], xv[0][1]), pk2(xv[0][2], xv[0][3])});
              if (act) st8(rs, dst + c1 * 2, (u32x2){pk2(xv[1][0], xv[1][1]), pk2(xv[1][2], xv[1][3])});
            } else {
              st8(rs, dst + c0 * 2, (u32x2){pk2(ln[0][0], ln[0][1]), pk2(ln[0][2], ln[0][3])});
              if (act) st8(rs, dst + c1 * 2, (u32x2){pk2(ln[1][0], ln[1][1]), pk2(ln[1][2], ln[1][3])});
            }
          }
          wait_vm0();
        }
        }
        if (l == 0) {
          __syncthreads();
          if (tid == 0) add_cnt(cnt + (C_XS2 + s) * 32);
        }
        TSW(tsb + 7);
        if (h == 0) TL(3072 + task * 4 + 2);
      }
    }
  }
}

__global__ void __launch_bounds__(256, 1) k_main(char* __restrict__ ws, const unsigned short* __restrict__ mem0, const unsigned short* __restrict__ xin,
                                                 unsigned short* __restrict__ out) {
  extern __shared__ __attribute__((aligned(16))) char smem[];
  const int b = blockIdx.x;
  TL(2048 + b);
  if ((b & 7) < CX) {
    const int q = (b >> 3) * CX + (b & 7);
    if (q < 2 * NH) role_S(ws, smem, q >> 1, q & 1);
    else if (q < NSV) role_V(ws, smem, (q - 2 * NH) >> 1, q & 1, mem0);
    else if (q < NSV + 16 * NCW) { const int k = q - NSV; role_W(ws, smem, k >> 4, k & 15, xin, out); }
  } else { const int k = 16 * NCW + (b >> 3) * (8 - CX) + (b & 7) - CX; role_W(ws, smem, k >> 4, k & 15, xin, out); }
  TL(2304 + b);
}

extern "C" void kernel_launch(void* const* d_in, const int* in_sizes, int n_in, void* d_out, int out_size, void* d_ws,
                              size_t ws_size, hipStream_t stream) {
  const unsigned short* A_q = (const unsigned short*)d_in[0];
  const unsigned short* A_k = (const unsigned short*)d_in[1];
  const unsigned short* A_v = (const unsigned short*)d_in[2];
  const unsigned short* A_shift = (const unsigned short*)d_in[3];
  const unsigned short* A_ssmk = (const unsigned short*)d_in[4];
  const unsigned short* A_D = (const unsigned short*)d_in[5];
  const unsigned short* B_mem0 = (const unsigned short*)d_in[7];
  const unsigned short* B_Wq = (const unsigned short*)d_in[8];
  const unsigned short* B_Wk = (const unsigned short*)d_in[9];
  const unsigned short* B_Wv = (const unsigned short*)d_in[10];
  const unsigned short* B_Wo = (const unsigned short*)d_in[11];
  char* ws = (char*)d_ws;
  constexpr int LDS_S = 41 * RS + 4 * 14 * 1024 + 4 * 16 * 44 * 4 + 16 * SPS + 4 * 14 * 384;
  constexpr int LDS_W = 48 * RS + 4 * 32 * 48 * 4 + 32 * 112 + 84 * 1024;
  constexpr int LDS_MAIN = LDS_S > LDS_W ? LDS_S : LDS_W;
  hipFuncSetAttribute((const void*)k_pre, hipFuncAttributeMaxDynamicSharedMemorySize, LDS_PRE);
  hipFuncSetAttribute((const void*)k_main, hipFuncAttributeMaxDynamicSharedMemorySize, LDS_MAIN);
  k_pre<<<224, 512, LDS_PRE, stream>>>(A_k, A_v, A_shift, A_ssmk, A_D, B_Wq, B_Wk, B_Wv, B_Wo, ws);
  k_main<<<256, 256, LDS_MAIN, stream>>>(ws, B_mem0, A_q, (unsigned short*)d_out);
}
```
